```python
import torch

def _softmax(x, axis=-1):
    return torch.softmax(x, dim=axis)
def _gelu(x):
    return torch.nn.functional.gelu(x, approximate="tanh")
def _silu(x):
    return torch.nn.functional.silu(x)
def _sigmoid(x):
    return torch.sigmoid(x)
def _layernorm(x, axis=-1, eps=1e-5):
    m = x.mean(dim=axis, keepdim=True)
    v = x.var(dim=axis, keepdim=True, unbiased=False)
    return (x - m) / torch.sqrt(v + eps)
def _scatter_add(tgt, idx, src, axis=0):
    return tgt.clone().index_add_(axis, idx, src)
def _segment_sum(src, seg, n):
    out = torch.zeros((n,) + src.shape[1:], dtype=src.dtype)
    return out.index_add_(0, seg, src)
def _pad(x, pw):
    flat = tuple(v for pair in reversed(pw) for v in pair)
    return torch.nn.functional.pad(x, flat)

kernel_name = 'sequential_preLN__sliding_block_cached__swiglu_ffn__decode'

def setup_inputs():

    _w = (lambda a: (lambda t: t.to(torch.bfloat16) if t.is_floating_point() else t)(torch.as_tensor(a)))
    def _A_setup():
        B, Sq, D = 4, 1, 448
        Hq, Hkv, Sk, Dh = 16, 8, 4096, 256
        assert Hq % Hkv == 0, (Hq, Hkv)
        return {
            "x": _w((lambda *s: torch.randn(*s, dtype=torch.bfloat16) * 0.05)(B, Sq, D)),
            "k_cache": _w((lambda *s: torch.randn(*s, dtype=torch.bfloat16) * 3.2)(B, Hkv, Sk, Dh)),
            "v_cache": _w((lambda *s: torch.randn(*s, dtype=torch.bfloat16) * 0.4)(B, Hkv, Sk, Dh)),
            "wq": _w((lambda *s: torch.randn(*s, dtype=torch.bfloat16) * 3.2)(D, Hq * Dh) * (D ** -0.5)),
            "wo": _w((lambda *s: torch.randn(*s, dtype=torch.bfloat16) * 0.4)(Hq * Dh, D) * ((Hq * Dh) ** -0.5)),
        }
    _A_in = _A_setup()

    def _B_setup():
        B, S, D, F = 4, 1, 448, 1792
        return {
            "x": _w((lambda *s: torch.randn(*s, dtype=torch.bfloat16) * 0.05)(B, S, D)),
            "w_gate": _w((lambda *s: torch.randn(*s, dtype=torch.bfloat16) * 0.05)(D, F)),
            "w_up": _w((lambda *s: torch.randn(*s, dtype=torch.bfloat16) * 0.05)(D, F)),
            "w_down": _w((lambda *s: torch.randn(*s, dtype=torch.bfloat16) * 0.05)(F, D)),
        }
    _B_in = _B_setup()

    return {
        "A_x": _A_in["x"],
        "A_k_cache": _A_in["k_cache"],
        "A_v_cache": _A_in["v_cache"],
        "A_wq": _A_in["wq"],
        "A_wo": _A_in["wo"],
        "B_x": _B_in["x"],
        "B_w_gate": _B_in["w_gate"],
        "B_w_up": _B_in["w_up"],
        "B_w_down": _B_in["w_down"],
    }

def reference(A_x, A_k_cache, A_v_cache, A_wq, A_wo, B_x, B_w_gate, B_w_up, B_w_down):
    def _A_ref(x, k_cache, v_cache, wq, wo):
        B, Sq, D = x.shape
        Hq, Dh = 16, 256
        Hkv, Sk = k_cache.shape[1], k_cache.shape[2]
        W = min(4096, max(256, Sk // 4))
        rep = Hq // Hkv
        q = (lambda x, *d: x.permute(*d))((x @ wq).reshape(B, Sq, Hq, Dh), 0, 2, 1, 3)
        k = (lambda x, n, axis: torch.repeat_interleave(x, n, dim=axis))(k_cache[:, :, Sk - W:, :], rep, axis=1)
        v = (lambda x, n, axis: torch.repeat_interleave(x, n, dim=axis))(v_cache[:, :, Sk - W:, :], rep, axis=1)
        scores = torch.einsum("bhqd,bhkd->bhqk", q, k) * (Dh ** -0.5)
        p = _softmax(scores, axis=-1)
        o = torch.einsum("bhqk,bhkd->bhqd", p, v)
        return (lambda x, *d: x.permute(*d))(o, 0, 2, 1, 3).reshape(B, Sq, Hq * Dh) @ wo

    def _B_ref(x, w_gate, w_up, w_down):
        g = _silu(x @ w_gate)
        u = x @ w_up
        return (g * u) @ w_down

    x = A_x
    h = x + _A_ref(_layernorm(x, axis=-1), A_k_cache, A_v_cache, A_wq, A_wo)
    h2 = h + _B_ref(_layernorm(h, axis=-1), B_w_gate, B_w_up, B_w_down)
    return h2
```

```cpp
#include <hip/hip_runtime.h>
#include <hip/hip_bf16.h>
#define MIMIC 0
typedef unsigned short u16;
__device__ __forceinline__ float bf2f(u16 b){ return __uint_as_float(((unsigned)b)<<16); }
__device__ __forceinline__ u16 f2bf(float f){ unsigned u=__float_as_uint(f); u += 0x7FFFu + ((u>>16)&1u); return (u16)(u>>16); }
__device__ __forceinline__ float rb(float f){
  return f;
}
constexpr int B=4,D=448,HQ=16,HKV=8,SK=4096,DH=256,W=1024,F=1792;

__device__ void ln_rows(const float* in, float* out, int tid, int nthr){
  if(tid<B){
    const float* r=in+tid*D; float s=0; for(int i=0;i<D;i++) s+=r[i];
    float mean=s/D; float v=0; for(int i=0;i<D;i++){ float d=r[i]-mean; v+=d*d; } v/=D;
    float m=rb(mean); v=rb(v); float den=rb(sqrtf(rb(v+1e-5f)));
    for(int i=0;i<D;i++) out[tid*D+i]=rb(rb(r[i]-m)/den);
  }
  __syncthreads();
}
__global__ void k_q(const u16* x,const u16* wq,float* q){
  __shared__ float xs[B*D], xn[B*D];
  int tid=threadIdx.x;
  for(int i=tid;i<B*D;i+=blockDim.x) xs[i]=bf2f(x[i]);
  __syncthreads();
  ln_rows(xs,xn,tid,blockDim.x);
  int col=blockIdx.x*blockDim.x+tid;
  float acc[B]={0,0,0,0};
  for(int k=0;k<D;k++){ float w=bf2f(wq[(size_t)k*4096+col]); for(int b=0;b<B;b++) acc[b]+=xn[b*D+k]*w; }
  for(int b=0;b<B;b++) q[b*4096+col]=rb(acc[b]);
}
__global__ void k_attn(const float* q,const u16* kc,const u16* vc,float* o){
  __shared__ float qs[DH], sc[W], red[256];
  int h=blockIdx.x%HQ, b=blockIdx.x/HQ, kv=h/2, tid=threadIdx.x;
  qs[tid]=q[b*4096+h*DH+tid];
  __syncthreads();
  const u16* K=kc+((size_t)(b*HKV+kv)*SK+(SK-W))*DH;
  const u16* V=vc+((size_t)(b*HKV+kv)*SK+(SK-W))*DH;
  float mx=-INFINITY;
  for(int j=tid;j<W;j+=256){ float s=0; for(int d=0;d<DH;d++) s+=qs[d]*bf2f(K[(size_t)j*DH+d]); s=rb(rb(s)*0.0625f); sc[j]=s; mx=fmaxf(mx,s);}
  red[tid]=mx; __syncthreads();
  for(int s=128;s>0;s>>=1){ if(tid<s) red[tid]=fmaxf(red[tid],red[tid+s]); __syncthreads(); }
  mx=red[0]; __syncthreads();
  float sum=0; for(int j=tid;j<W;j+=256){ float e=expf(sc[j]-mx); sc[j]=e; sum+=e; }
  red[tid]=sum; __syncthreads();
  for(int s=128;s>0;s>>=1){ if(tid<s) red[tid]+=red[tid+s]; __syncthreads(); }
  sum=red[0]; __syncthreads();
  for(int j=tid;j<W;j+=256) sc[j]=rb(sc[j]/sum);
  __syncthreads();
  float acc=0; for(int j=0;j<W;j++) acc+=sc[j]*bf2f(V[(size_t)j*DH+tid]);
  o[b*4096+h*DH+tid]=rb(acc);
}
__global__ void k_wo(const float* o,const u16* wo,const u16* x,float* h){
  int b=blockIdx.x, c=threadIdx.x;
  float acc=0; for(int k=0;k<4096;k++) acc+=o[b*4096+k]*bf2f(wo[(size_t)k*D+c]);
  h[b*D+c]=rb(bf2f(x[b*D+c])+rb(acc));
}
__global__ void k_gu(const float* h,const u16* wg,const u16* wu,float* act){
  __shared__ float xs[B*D], xn[B*D];
  int tid=threadIdx.x;
  for(int i=tid;i<B*D;i+=blockDim.x) xs[i]=h[i];
  __syncthreads();
  ln_rows(xs,xn,tid,blockDim.x);
  int col=blockIdx.x*blockDim.x+tid;
  float g[B]={0,0,0,0},u[B]={0,0,0,0};
  for(int k=0;k<D;k++){ float a=bf2f(wg[(size_t)k*F+col]), c=bf2f(wu[(size_t)k*F+col]); for(int b=0;b<B;b++){ g[b]+=xn[b*D+k]*a; u[b]+=xn[b*D+k]*c; } }
  for(int b=0;b<B;b++){ float gg=rb(g[b]); float s=rb(gg/(1.f+expf(-gg))); act[b*F+col]=rb(s*rb(u[b])); }
}
__global__ void k_down(const float* act,const u16* wd,const float* h,u16* out){
  int b=blockIdx.x, c=threadIdx.x;
  float acc=0; for(int k=0;k<F;k++) acc+=act[b*F+k]*bf2f(wd[(size_t)k*D+c]);
  out[b*D+c]=f2bf(h[b*D+c]+rb(acc));
}
extern "C" void kernel_launch(void* const* d_in, const int* in_sizes, int n_in, void* d_out, int out_size, void* d_ws, size_t ws_size, hipStream_t stream){
  const u16* x=(const u16*)d_in[0]; const u16* kc=(const u16*)d_in[1]; const u16* vc=(const u16*)d_in[2];
  const u16* wq=(const u16*)d_in[3]; const u16* wo=(const u16*)d_in[4];
  const u16* wg=(const u16*)d_in[6]; const u16* wu=(const u16*)d_in[7]; const u16* wd=(const u16*)d_in[8];
  float* ws=(float*)d_ws; float* q=ws; float* o=ws+16384; float* h=ws+32768; float* act=ws+40960;
  k_q<<<64,64,0,stream>>>(x,wq,q);
  k_attn<<<64,256,0,stream>>>(q,kc,vc,o);
  k_wo<<<4,448,0,stream>>>(o,wo,x,h);
  k_gu<<<28,64,0,stream>>>(h,wg,wu,act);
  k_down<<<4,448,0,stream>>>(act,wd,h,(u16*)d_out);
}
```

```cpp
#include <hip/hip_runtime.h>
#include <hip/hip_bf16.h>

typedef unsigned short u16;
typedef unsigned long long u64;
typedef unsigned v4u __attribute__((ext_vector_type(4)));
typedef unsigned v2u __attribute__((ext_vector_type(2)));
typedef float f32x4 __attribute__((ext_vector_type(4)));
typedef float f32x2 __attribute__((ext_vector_type(2)));
typedef short s16x4 __attribute__((ext_vector_type(4)));
typedef __bf16 bf16x8 __attribute__((ext_vector_type(8)));
typedef __bf16 bf16x2 __attribute__((ext_vector_type(2)));
#define GAS __attribute__((address_space(1)))
#define LAS __attribute__((address_space(3)))

constexpr int D = 448, SK = 4096, WIN = 1024, FF = 1792;
constexpr int WS_EPOCH = 0;
constexpr int WS_GQ = 4096;
constexpr int WS_GS = WS_GQ + 8 * 4 * 4 * 512 * 8;
constexpr int WS_GC = WS_GS + 8 * 32 * 4 * 8;
constexpr int WS_G1 = WS_GC + 8 * 8 * 32 * 64 * 8;
constexpr int WS_G2 = WS_G1 + 112 * 64 * 16 * 8;
constexpr int WS_G3 = WS_G2 + 8192;
constexpr int WS_END = WS_G3 + 112 * 56 * 16 * 8;
constexpr int WS_STAMP = 8 << 20;
constexpr int L_K = 0, L_V = 65536, L_WQ = 131072, L_XN = L_WQ + 14336, XN_STR = 912;
constexpr int L_Q = L_XN + 3712, Q_STR = 576;
constexpr int L_SC = L_Q + 1152;
constexpr int L_P = L_SC + 512, P_STR = 320;
constexpr int L_OB = L_P + 8 * 640, OB_STR = 192;
constexpr int L_ACT = L_OB + 768;
constexpr int L_END = L_ACT + 256;
static_assert(L_END <= 163840, "LDS");
constexpr unsigned SPIN_CAP = 1u << 16;
constexpr int SC1 = 16;
constexpr float LOG2E = 1.4426950408889634f;

struct Params {
  const u16 *x, *kc, *vc, *wq, *wo, *wg, *wu, *wd;
  u16* out;
  unsigned char* ws;
};

#define SB() __builtin_amdgcn_sched_barrier(0)
#define MEMFENCE() asm volatile("" ::: "memory")
#define WG_BARRIER() asm volatile("s_waitcnt lgkmcnt(0)\n\ts_barrier" ::: "memory")
#define LDS_WAIT() asm volatile("s_waitcnt lgkmcnt(0)" ::: "memory")

#define STAMP(i) do {} while (0)
#define STAMP_DECL do {} while (0)
#define STAMP_FLUSH() do {} while (0)

__device__ __forceinline__ v4u ldnt(const void* q) { return __builtin_nontemporal_load((const GAS v4u*)q); }
__device__ __forceinline__ float bflo(unsigned u) { return __builtin_bit_cast(float, u << 16); }
__device__ __forceinline__ float bfhi(unsigned u) { return __builtin_bit_cast(float, u & 0xffff0000u); }
__device__ __forceinline__ unsigned pk(float lo, float hi) {
  f32x2 v = {lo, hi};
  return __builtin_bit_cast(unsigned, __builtin_convertvector(v, bf16x2));
}
__device__ __forceinline__ float rbf(float f) { return bflo(pk(f, f)); }
template <int CTRL> __device__ __forceinline__ float dpp(float x) {
  return __builtin_bit_cast(float, __builtin_amdgcn_mov_dpp(__builtin_bit_cast(int, x), CTRL, 0xf, 0xf, true));
}
constexpr int ROR1 = 0x121, ROR2 = 0x122, ROR4 = 0x124, ROR8 = 0x128;
__device__ __forceinline__ float x16_sum(float x) {
  auto s = __builtin_amdgcn_permlane16_swap(__float_as_uint(x), __float_as_uint(x), false, false);
  return __uint_as_float(s[0]) + __uint_as_float(s[1]);
}
__device__ __forceinline__ float x32_sum(float x) {
  auto s = __builtin_amdgcn_permlane32_swap(__float_as_uint(x), __float_as_uint(x), false, false);
  return __uint_as_float(s[0]) + __uint_as_float(s[1]);
}
__device__ __forceinline__ float x16_max(float x) {
  auto s = __builtin_amdgcn_permlane16_swap(__float_as_uint(x), __float_as_uint(x), false, false);
  return fmaxf(__uint_as_float(s[0]), __uint_as_float(s[1]));
}
__device__ __forceinline__ float x32_max(float x) {
  auto s = __builtin_amdgcn_permlane32_swap(__float_as_uint(x), __float_as_uint(x), false, false);
  return fmaxf(__uint_as_float(s[0]), __uint_as_float(s[1]));
}
__device__ __forceinline__ float wave_sum(float x) {
  x += dpp<ROR8>(x); x += dpp<ROR4>(x); x += dpp<ROR2>(x); x += dpp<ROR1>(x);
  return x32_sum(x16_sum(x));
}
__device__ __forceinline__ float wave_max(float x) {
  x = fmaxf(x, dpp<ROR8>(x)); x = fmaxf(x, dpp<ROR4>(x)); x = fmaxf(x, dpp<ROR2>(x)); x = fmaxf(x, dpp<ROR1>(x));
  return x32_max(x16_max(x));
}
__device__ __forceinline__ f32x4 mfma32(v4u a, v4u b, f32x4 c) {
  return __builtin_amdgcn_mfma_f32_16x16x32_bf16(__builtin_bit_cast(bf16x8, a), __builtin_bit_cast(bf16x8, b), c, 0, 0, 0);
}
__device__ __forceinline__ f32x4 mfma16(v2u a, v2u b, f32x4 c) {
  return __builtin_amdgcn_mfma_f32_16x16x16bf16_1k(__builtin_bit_cast(s16x4, a), __builtin_bit_cast(s16x4, b), c, 0, 0, 0);
}
__device__ __forceinline__ v2u trd(LAS unsigned char* a) {
  return __builtin_bit_cast(v2u, __builtin_amdgcn_ds_read_tr16_b64_v4i16((LAS s16x4*)a));
}
__device__ __forceinline__ v4u trd2(LAS unsigned char* a0, LAS unsigned char* a1) {
  const v2u lo = trd(a0), hi = trd(a1);
  return v4u{lo.x, lo.y, hi.x, hi.y};
}
__device__ __forceinline__ v4u layernorm_row(v4u v, bool act) {
  float f[8];
#pragma unroll
  for (int i = 0; i < 4; ++i) { f[2 * i] = bflo(v[i]); f[2 * i + 1] = bfhi(v[i]); }
  const float x0 = __builtin_bit_cast(float, __builtin_amdgcn_readfirstlane(__builtin_bit_cast(int, f[0])));
  float s1 = 0.f, s2 = 0.f;
#pragma unroll
  for (int i = 0; i < 8; ++i) { const float d = f[i] - x0; s1 += d; s2 = fmaf(d, d, s2); }
  s1 = act ? s1 : 0.f; s2 = act ? s2 : 0.f;
  s1 = wave_sum(s1); s2 = wave_sum(s2);
  const float md = s1 * (1.0f / D);
  const float var = fmaxf(fmaf(-md, md, s2 * (1.0f / D)), 0.f);
  const float m = rbf(x0 + md);
  const float den = rbf(__builtin_amdgcn_sqrtf(rbf(rbf(var) + 1e-5f)));
  const float inv = __builtin_amdgcn_rcpf(den);
  v4u o;
#pragma unroll
  for (int i = 0; i < 4; ++i) {
    const unsigned d = pk(f[2 * i] - m, f[2 * i + 1] - m);
    o[i] = pk(bflo(d) * inv, bfhi(d) * inv);
  }
  return o;
}
__device__ __forceinline__ f32x4 sel4(int sub, f32x4 a0, f32x4 a1, f32x4 a2, f32x4 a3) {
  const f32x4 lo = (sub & 1) ? a1 : a0, hi = (sub & 1) ? a3 : a2;
  return (sub & 2) ? hi : lo;
}
__device__ __forceinline__ v4u gran2(float a, float b, unsigned tag) { return v4u{__float_as_uint(a), tag, __float_as_uint(b), tag}; }

__global__ void __launch_bounds__(512, 2) fused_decode_block(Params p) {
  __shared__ __attribute__((aligned(16))) unsigned char smem[L_END];
  LAS unsigned char* L = (LAS unsigned char*)smem;
  const int c = blockIdx.x, tid = threadIdx.x, lane = tid & 63, w = __builtin_amdgcn_readfirstlane(tid >> 6);
  const int kvh = c & 7, j = c >> 3, b = j >> 3, s = j & 7;
  const int n = lane & 15, g = lane >> 4;
  const __amdgpu_buffer_rsrc_t rs = __builtin_amdgcn_make_buffer_rsrc((void*)p.ws, 0, WS_END, 0x00020000);
  STAMP_DECL;
  STAMP(0);

  const unsigned ep = __builtin_amdgcn_raw_buffer_load_b32(rs, WS_EPOCH, 0, SC1);
  const v4u xr = *(const GAS v4u*)(p.x + (w & 3) * D + (lane < 56 ? lane : 55) * 8);
  const int w7 = w < 7 ? w : 6;
  const char* wqp = (const char*)p.wq + (size_t)(b * 112 + w7 * 16 + (lane >> 3)) * 8192 + (kvh * 512 + s * 64) * 2 + (lane & 7) * 16;
  const v4u wq0 = ldnt(wqp), wq1 = ldnt(wqp + 8 * 8192);
  SB();
  const size_t kvoff = ((size_t)(b * 8 + kvh) * SK + (SK - WIN)) * 512 + (size_t)(s + 8 * w) * 1024 + lane * 16;
  const char* kp = (const char*)p.kc + kvoff;
  const char* vp = (const char*)p.vc + kvoff;
  v4u K[8], V[8];
#pragma unroll
  for (int i = 0; i < 4; ++i) K[i] = ldnt(kp + i * 65536);
  SB();
  const unsigned tag = __builtin_amdgcn_readfirstlane(ep) + 1u;

  if (w >= 4) {
    const v4u xn = layernorm_row(xr, lane < 56);
    if (lane < 56) *(LAS v4u*)(L + L_XN + (w & 3) * XN_STR + lane * 16) = xn;
  }
  if (w < 7) {
    const int r0 = w * 16 + (lane >> 3), ch = lane & 7;
    *(LAS v4u*)(L + L_WQ + r0 * 128 + ((ch ^ (((r0 >> 1) & 3) << 1)) << 4)) = wq0;
    *(LAS v4u*)(L + L_WQ + (r0 + 8) * 128 + ((ch ^ (((r0 >> 1) & 3) << 1)) << 4)) = wq1;
  }
  STAMP(1);
  WG_BARRIER();
  if (w < 2) {
    f32x4 a0 = {0.f, 0.f, 0.f, 0.f}, a1 = a0;
    const int row = 4 * g + (n >> 2), sw = (row >> 1) & 3;
    LAS unsigned char* t0 = L + L_WQ + row * 128 + (((2 * w) ^ sw) << 5) + (n & 3) * 8;
    LAS unsigned char* t1 = L + L_WQ + row * 128 + (((2 * w + 1) ^ sw) << 5) + (n & 3) * 8;
    LAS unsigned char* xb = L + L_XN + (n >> 2) * XN_STR + (b * 112 + 4 * g) * 2;
#pragma unroll
    for (int ks = 0; ks < 7; ++ks) {
      const v2u xf = *(LAS v2u*)(xb + ks * 32);
      a0 = mfma16(trd(t0 + ks * 2048), xf, a0);
      a1 = mfma16(trd(t1 + ks * 2048), xf, a1);
    }
    const f32x4 a = (n & 2) ? a1 : a0;
    const float e0 = (n & 1) ? a[2] : a[0], e1 = (n & 1) ? a[3] : a[1];
    const int off = WS_GQ + (((kvh * 4 + (n >> 2)) * 4 + b) * 256 + (s * 2 + w) * 16 + g * 4 + (n & 3)) * 16;
    __builtin_amdgcn_raw_buffer_store_b128(gran2(e0, e1, tag), rs, off, 0, SC1);
  }
  STAMP(2);

  LAS unsigned char* kw = L + L_K + w * 8192;
  const int kch = lane & 31, khalf = lane >> 5;
#pragma unroll
  for (int i = 0; i < 4; ++i) {
    const int key = 2 * i + khalf;
    *(LAS v4u*)(kw + key * 512 + (((kch & 16) | ((kch ^ key) & 15)) << 4)) = K[i];
    SB();
    K[4 + i] = ldnt(kp + (4 + i) * 65536);
    SB();
  }
  const int gqoff = WS_GQ + ((kvh * 4 + b) * 4) * 4096 + tid * 8;
  v2u qg[4];
#pragma unroll
  for (int i = 4; i < 8; ++i) {
    const int key = 2 * i + khalf;
    *(LAS v4u*)(kw + key * 512 + (((kch & 16) | ((kch ^ key) & 15)) << 4)) = K[i];
    SB();
    V[i - 4] = ldnt(vp + (i - 4) * 65536);
    SB();
    if (i == 4) {
#pragma unroll
      for (int k = 0; k < 4; ++k) qg[k] = __builtin_amdgcn_raw_buffer_load_b64(rs, gqoff + k * 4096, 0, SC1);
      SB();
    }
  }
  STAMP(3);
  float qv = (__uint_as_float(qg[0].x) + __uint_as_float(qg[1].x)) + (__uint_as_float(qg[2].x) + __uint_as_float(qg[3].x));
  if (__builtin_expect(!__all((qg[0].y == tag) & (qg[1].y == tag) & (qg[2].y == tag) & (qg[3].y == tag)), 0)) {
    for (unsigned spins = 0;; ) {
      MEMFENCE();
#pragma unroll
      for (int k = 0; k < 4; ++k) qg[k] = __builtin_amdgcn_raw_buffer_load_b64(rs, gqoff + k * 4096, 0, SC1);
      qv = (__uint_as_float(qg[0].x) + __uint_as_float(qg[1].x)) + (__uint_as_float(qg[2].x) + __uint_as_float(qg[3].x));
      const bool ok = (qg[0].y == tag) & (qg[1].y == tag) & (qg[2].y == tag) & (qg[3].y == tag);
      if (__all(ok) || ++spins > SPIN_CAP) break;
    }
  }
  {
    const int pos = tid >> 1, sub = pos & 3, qq = (pos >> 2) & 3, tp = pos >> 4;
    const int col = 32 * tp + 16 * (sub >> 1) + 4 * qq + 2 * (sub & 1) + (tid & 1);
    *(LAS u16*)(L + L_Q + (col >> 8) * Q_STR + (col & 255) * 2) = (u16)pk(qv, qv);
  }
  SB();
  V[4] = ldnt(vp + 4 * 65536);
  SB();
  STAMP(4);
  WG_BARRIER();
  {
    f32x4 a = {0.f, 0.f, 0.f, 0.f};
#pragma unroll
    for (int t = 0; t < 8; ++t) {
      const int ch = 4 * t + g;
      const v4u kf = *(LAS v4u*)(kw + n * 512 + (((ch & 16) | ((ch ^ n) & 15)) << 4));
      const v4u qf = *(LAS v4u*)(L + L_Q + (n & 1) * Q_STR + ch * 16);
      a = mfma32(qf, kf, a);
    }
    if (lane < 16) *(LAS unsigned*)(L + L_SC + (16 * (n >> 1) + 2 * w + (n & 1)) * 4) = pk(a[0], a[1]);
  }
  WG_BARRIER();
  const int vch = lane & 31;
#define STAGE_V(i) do { const int row_ = 16 * (i) + 2 * w + khalf; \
    *(LAS v4u*)(L + L_V + row_ * 512 + ((vch ^ ((((row_ & 3) << 1) | ((row_ >> 3) & 1)) << 1)) << 4)) = V[i]; } while (0)
  STAGE_V(0); STAGE_V(1);
  SB();
  V[5] = ldnt(vp + 5 * 65536);
  SB();
  LAS unsigned char* pw = L + L_P + w * 640;
  {
    const v2u sc = *(LAS v2u*)(L + L_SC + lane * 8);
    const float s00 = bflo(sc.x) * 0.0625f, s10 = bfhi(sc.x) * 0.0625f, s01 = bflo(sc.y) * 0.0625f, s11 = bfhi(sc.y) * 0.0625f;
    const float m0 = wave_max(fmaxf(s00, s01)), m1 = wave_max(fmaxf(s10, s11));
    const float e00 = __builtin_amdgcn_exp2f((s00 - m0) * LOG2E), e01 = __builtin_amdgcn_exp2f((s01 - m0) * LOG2E);
    const float e10 = __builtin_amdgcn_exp2f((s10 - m1) * LOG2E), e11 = __builtin_amdgcn_exp2f((s11 - m1) * LOG2E);
    *(LAS unsigned*)(pw + lane * 4) = pk(e00, e01);
    *(LAS unsigned*)(pw + P_STR + lane * 4) = pk(e10, e11);
    if (w == 7) {
      const float l0 = wave_sum(e00 + e01), l1 = wave_sum(e10 + e11);
      if (lane < 2) __builtin_amdgcn_raw_buffer_store_b128(gran2(lane ? m1 : m0, lane ? l1 : l0, tag), rs, WS_GS + (kvh * 32 + j) * 32 + lane * 16, 0, SC1);
    }
  }
  STAMP(5);
  STAGE_V(2);
  SB();
  V[6] = ldnt(vp + 6 * 65536);
  SB();
  STAGE_V(3);
  SB();
  V[7] = ldnt(vp + 7 * 65536);
  SB();
  STAGE_V(4); STAGE_V(5);
  v4u pf[4];
#pragma unroll
  for (int t = 0; t < 4; ++t) pf[t] = *(LAS v4u*)(pw + (n & 1) * P_STR + t * 64 + g * 16);
  WG_BARRIER();
  f32x4 o0 = {0.f, 0.f, 0.f, 0.f}, o1 = o0;
  const int vq = n >> 2, vsw = (vq << 1) | (g & 1);
  LAS unsigned char* v0 = L + L_V + (8 * g + vq) * 512 + (((2 * w) ^ vsw) << 5) + (n & 3) * 8;
  LAS unsigned char* v1 = L + L_V + (8 * g + vq) * 512 + (((2 * w + 1) ^ vsw) << 5) + (n & 3) * 8;
#pragma unroll
  for (int t = 0; t < 3; ++t) {
    o0 = mfma32(trd2(v0 + t * 16384, v0 + t * 16384 + 2048), pf[t], o0);
    o1 = mfma32(trd2(v1 + t * 16384, v1 + t * 16384 + 2048), pf[t], o1);
  }
  SB();
  STAGE_V(6); STAGE_V(7);
  STAMP(6);
  WG_BARRIER();
  o0 = mfma32(trd2(v0 + 3 * 16384, v0 + 3 * 16384 + 2048), pf[3], o0);
  o1 = mfma32(trd2(v1 + 3 * 16384, v1 + 3 * 16384 + 2048), pf[3], o1);
  {
    const f32x4 a = (n & 2) ? o1 : o0;
    const float e0 = (n & 4) ? a[2] : a[0], e1 = (n & 4) ? a[3] : a[1];
    if (n < 8) __builtin_amdgcn_raw_buffer_store_b128(gran2(e0, e1, tag), rs, WS_GC + ((kvh * 8 + w) * 32 + j) * 512 + (g * 8 + n) * 16, 0, SC1);
  }
  STAMP(7);

  const int lrow = lane >> 3, lch = lane & 7;
  const int tsw = ((vq >> 1) & 1) | ((g & 1) << 1);
#define LT_WADDR(base, row) ((base) + (row) * 128 + ((lch ^ (((((row) >> 1) & 1) | ((((row) >> 3) & 1) << 1)) << 1)) << 4))
#define LT_RADDR(base, ks, t, h) ((base) + (32 * (ks) + 8 * g + 4 * (h) + vq) * 128 + (((t) ^ tsw) << 5) + (n & 3) * 8)

  if (j >= 8) {
    const int rsl = j & 7, cg = (j >> 3) - 1;
    const int nline = cg == 0 ? 3 : 2, line0 = cg == 0 ? 0 : 2 * cg + 1;
    v4u wf[2][4];
    if (w >= 4 && w - 4 < nline) {
      const char* wop = (const char*)p.wo + (size_t)(kvh * 512 + rsl * 32 + lrow) * 896 + (line0 + w - 4) * 128 + lch * 16;
      v4u t[8];
#pragma unroll
      for (int i = 0; i < 8; ++i) t[i] = ldnt(wop + ((i & 3) * 8 + (i >> 2) * 256) * 896);
      SB();
#pragma unroll
      for (int i = 0; i < 8; ++i) *(LAS v4u*)LT_WADDR(kw, 8 * i + lrow) = t[i];
      LDS_WAIT();
#pragma unroll
      for (int ks = 0; ks < 2; ++ks)
#pragma unroll
        for (int t4 = 0; t4 < 4; ++t4) wf[ks][t4] = trd2(LT_RADDR(kw, ks, t4, 0), LT_RADDR(kw, ks, t4, 1));
    } else {
#pragma unroll
      for (int ks = 0; ks < 2; ++ks)
#pragma unroll
        for (int t4 = 0; t4 < 4; ++t4) wf[ks][t4] = v4u{0u, 0u, 0u, 0u};
    }
    if (w < 4) {
      v4u st;
      for (unsigned spins = 0;; ) {
        MEMFENCE();
        st = __builtin_amdgcn_raw_buffer_load_b128(rs, WS_GS + kvh * 1024 + lane * 16, 0, SC1);
        if (__all((st.y == tag) & (st.w == tag)) || ++spins > SPIN_CAP) break;
      }
      const float m = __uint_as_float(st.x), l = __uint_as_float(st.z);
      float M = fmaxf(m, dpp<ROR2>(m)); M = fmaxf(M, dpp<ROR4>(M)); M = fmaxf(M, dpp<ROR8>(M));
      const float e = __builtin_amdgcn_exp2f((m - M) * LOG2E);
      float Ls = l * e; Ls += dpp<ROR2>(Ls); Ls += dpp<ROR4>(Ls); Ls += dpp<ROR8>(Ls);
      const float wt = e * __builtin_amdgcn_rcpf(Ls);
      float cw[4];
#pragma unroll
      for (int u = 0; u < 4; ++u) cw[u] = __shfl(wt, 16 * w + 4 * u + 2 * khalf + (lane & 1));
      v4u v[4];
      const int off = WS_GC + ((kvh * 8 + rsl) * 32 + 8 * w) * 512 + lane * 16;
      for (unsigned spins = 0;; ) {
        MEMFENCE();
        bool ok = true;
#pragma unroll
        for (int u = 0; u < 4; ++u) v[u] = __builtin_amdgcn_raw_buffer_load_b128(rs, off + u * 1024, 0, SC1);
#pragma unroll
        for (int u = 0; u < 4; ++u) ok &= (v[u].y == tag) & (v[u].w == tag);
        if (__all(ok) || ++spins > SPIN_CAP) break;
      }
      float a0 = 0.f, a1 = 0.f;
#pragma unroll
      for (int u = 0; u < 4; ++u) { a0 = fmaf(cw[u], __uint_as_float(v[u].x), a0); a1 = fmaf(cw[u], __uint_as_float(v[u].z), a1); }
      a0 = x32_sum(a0); a1 = x32_sum(a1);
      const int pn = lane & 7, pq = (lane >> 3) & 3;
      const int kap = (pn & 1) * 32 + 16 * ((pn >> 1) & 1) + 4 * pq + 2 * (pn >> 2);
      if (lane < 32) *(LAS unsigned*)(L + L_OB + w * OB_STR + kap * 2) = pk(a0, a1);
    }
    STAMP(8);
    WG_BARRIER();
    if (w >= 4 && w - 4 < nline) {
      const v4u b0 = *(LAS v4u*)(L + L_OB + vq * OB_STR + g * 16), b1 = *(LAS v4u*)(L + L_OB + vq * OB_STR + 64 + g * 16);
      f32x4 acc[4];
#pragma unroll
      for (int t4 = 0; t4 < 4; ++t4) acc[t4] = mfma32(wf[0][t4], b0, f32x4{0.f, 0.f, 0.f, 0.f});
#pragma unroll
      for (int t4 = 0; t4 < 4; ++t4) acc[t4] = mfma32(wf[1][t4], b1, acc[t4]);
      const f32x4 a = sel4(n & 3, acc[0], acc[1], acc[2], acc[3]);
      const int off = WS_G1 + (((vq * 7 + line0 + w - 4) * 4 + g) * 64 + kvh * 8 + rsl) * 128 + (n & 3) * 16;
      __builtin_amdgcn_raw_buffer_store_b128(gran2(a[0], a[1], tag), rs, off, 0, SC1);
      __builtin_amdgcn_raw_buffer_store_b128(gran2(a[2], a[3], tag), rs, off + 64, 0, SC1);
    }
    STAMP(9);
    if (w == 7 && j >= 16 && j < 30) {
      const int r = (j - 16) * 8 + kvh;
      const int rq = r & 3, rl = (r >> 2) % 7, rb = r / 28;
      const int col = 64 * rl + 16 * (lane & 3) + 4 * rq + 2 * ((lane >> 2) & 1);
      const unsigned xin = *(const GAS unsigned*)(p.x + rb * D + col);
      float a0, a1;
      for (unsigned spins = 0;; ) {
        MEMFENCE();
        v4u v[8]; bool ok = true;
#pragma unroll
        for (int i = 0; i < 8; ++i) v[i] = __builtin_amdgcn_raw_buffer_load_b128(rs, WS_G1 + r * 8192 + i * 1024 + lane * 16, 0, SC1);
        a0 = 0.f; a1 = 0.f;
#pragma unroll
        for (int i = 0; i < 8; ++i) { ok &= (v[i].y == tag) & (v[i].w == tag); a0 += __uint_as_float(v[i].x); a1 += __uint_as_float(v[i].z); }
        if (__all(ok) || ++spins > SPIN_CAP) break;
      }
      a0 += dpp<ROR8>(a0); a1 += dpp<ROR8>(a1);
      a0 = x32_sum(x16_sum(a0)); a1 = x32_sum(x16_sum(a1));
      const unsigned at = pk(a0, a1);
      const unsigned h = pk(bflo(xin) + bflo(at), bfhi(xin) + bfhi(at));
      if (lane < 8) __builtin_amdgcn_raw_buffer_store_b64(v2u{h, tag}, rs, WS_G2 + r * 64 + lane * 8, 0, SC1);
      if (r == 0 && lane == 0) __builtin_amdgcn_raw_buffer_store_b32(tag, rs, WS_EPOCH, 0, SC1);
      STAMP(10);
      for (unsigned spins = 0;; ) {
        MEMFENCE();
        v4u v[7]; bool ok = true;
#pragma unroll
        for (int i = 0; i < 7; ++i) v[i] = __builtin_amdgcn_raw_buffer_load_b128(rs, WS_G3 + r * 7168 + i * 1024 + lane * 16, 0, SC1);
        a0 = 0.f; a1 = 0.f;
#pragma unroll
        for (int i = 0; i < 7; ++i) { ok &= (v[i].y == tag) & (v[i].w == tag); a0 += __uint_as_float(v[i].x); a1 += __uint_as_float(v[i].z); }
        if (__all(ok) || ++spins > SPIN_CAP) break;
      }
      a0 += dpp<ROR8>(a0); a1 += dpp<ROR8>(a1);
      a0 = x32_sum(x16_sum(a0)); a1 = x32_sum(x16_sum(a1));
      const unsigned ff = pk(a0, a1);
      if (lane < 8) *(GAS unsigned*)(p.out + rb * D + col) = pk(bflo(h) + bflo(ff), bfhi(h) + bfhi(ff));
      STAMP(11);
    }
  } else if (j < 7) {
    const int f = kvh * 7 + j;
    v4u df[4];
    WG_BARRIER();
    if (w < 7) {
      const size_t go = (size_t)(w * 64 + (lane >> 2)) * (FF * 2) + f * 64 + (lane & 3) * 16;
      const char* wdp = (const char*)p.wd + (size_t)(f * 32 + lrow) * 896 + w * 128 + lch * 16;
      v4u tg[4], tu[4], td[4];
#pragma unroll
      for (int i = 0; i < 4; ++i) { tg[i] = ldnt((const char*)p.wg + go + (size_t)i * 16 * FF * 2); tu[i] = ldnt((const char*)p.wu + go + (size_t)i * 16 * FF * 2); }
#pragma unroll
      for (int i = 0; i < 4; ++i) td[i] = ldnt(wdp + i * 8 * 896);
      SB();
#pragma unroll
      for (int i = 0; i < 4; ++i) {
        const int row = w * 64 + i * 16 + (lane >> 2), m = ((row >> 1) & 1) | (((row >> 3) & 1) << 1);
        *(LAS v4u*)(L + L_K + row * 128 + (((lane & 3) ^ m) << 4)) = tg[i];
        *(LAS v4u*)(L + L_K + row * 128 + 64 + (((lane & 3) ^ m) << 4)) = tu[i];
      }
      LAS unsigned char* dw = L + L_V + w * 4096;
#pragma unroll
      for (int i = 0; i < 4; ++i) *(LAS v4u*)LT_WADDR(dw, 8 * i + lrow) = td[i];
      LDS_WAIT();
#pragma unroll
      for (int t4 = 0; t4 < 4; ++t4) df[t4] = trd2(LT_RADDR(dw, 0, t4, 0), LT_RADDR(dw, 0, t4, 1));
    } else {
#pragma unroll
      for (int t4 = 0; t4 < 4; ++t4) df[t4] = v4u{0u, 0u, 0u, 0u};
    }
    WG_BARRIER();
    STAMP(8);
    v4u gf[14];
    if (w < 4) {
      LAS unsigned char* gb = L + L_K + (8 * g + vq) * 128 + ((n & 2) << 5) + ((w ^ tsw) << 4) + (n & 1) * 8;
#pragma unroll
      for (int ks = 0; ks < 14; ++ks) gf[ks] = trd2(gb + ks * 4096, gb + ks * 4096 + 512);
    } else {
#pragma unroll
      for (int ks = 0; ks < 14; ++ks) gf[ks] = v4u{0u, 0u, 0u, 0u};
      const int l56 = lane < 56 ? lane : 55;
      v4u h0, h1;
      for (unsigned spins = 0;; ) {
        MEMFENCE();
        h0 = __builtin_amdgcn_raw_buffer_load_b128(rs, WS_G2 + (w - 4) * 1792 + l56 * 16, 0, SC1);
        h1 = __builtin_amdgcn_raw_buffer_load_b128(rs, WS_G2 + (w - 4) * 1792 + 896 + l56 * 16, 0, SC1);
        if (__all((h0.y == tag) & (h0.w == tag) & (h1.y == tag) & (h1.w == tag)) || ++spins > SPIN_CAP) break;
      }
      STAMP(9);
      const v4u hn = layernorm_row(v4u{h0.x, h0.z, h1.x, h1.z}, lane < 56);
      if (lane < 56) {
        LAS unsigned char* hb = L + L_XN + (w - 4) * XN_STR;
        const int p0 = lane, p1 = lane + 56;
        const int c0 = 64 * (p0 >> 4) + 4 * ((p0 >> 2) & 3) + 2 * ((p0 >> 1) & 1) + 32 * (p0 & 1);
        const int c1 = 64 * (p1 >> 4) + 4 * ((p1 >> 2) & 3) + 2 * ((p1 >> 1) & 1) + 32 * (p1 & 1);
        *(LAS unsigned*)(hb + c0 * 2) = hn.x; *(LAS unsigned*)(hb + c0 * 2 + 32) = hn.y;
        *(LAS unsigned*)(hb + c1 * 2) = hn.z; *(LAS unsigned*)(hb + c1 * 2 + 32) = hn.w;
      }
    }
    WG_BARRIER();
    if (w < 4) {
      f32x4 a = {0.f, 0.f, 0.f, 0.f};
      LAS unsigned char* hb = L + L_XN + vq * XN_STR + g * 16;
#pragma unroll
      for (int ks = 0; ks < 14; ++ks) a = mfma32(gf[ks], *(LAS v4u*)(hb + ks * 64), a);
      const float lo = (n & 1) ? a[1] : a[0], hi = (n & 1) ? a[3] : a[2];
      const float mine = rbf((n & 2) ? hi : lo);
      auto sw = __builtin_amdgcn_permlane32_swap(__float_as_uint(mine), __float_as_uint(mine), false, false);
      const float gt = __uint_as_float(sw[0]), up = __uint_as_float(sw[1]);
      const float sg = rbf(gt * __builtin_amdgcn_rcpf(1.0f + __builtin_amdgcn_exp2f(-gt * LOG2E)));
      const float av = sg * up;
      if (lane < 32) *(LAS u16*)(L + L_ACT + vq * 64 + (8 * w + 4 * g + (n & 3)) * 2) = (u16)pk(av, av);
    }
    WG_BARRIER();
    if (w < 7) {
      const v4u af = *(LAS v4u*)(L + L_ACT + vq * 64 + g * 16);
      f32x4 acc[4];
#pragma unroll
      for (int t4 = 0; t4 < 4; ++t4) acc[t4] = mfma32(df[t4], af, f32x4{0.f, 0.f, 0.f, 0.f});
      const f32x4 a = sel4(n & 3, acc[0], acc[1], acc[2], acc[3]);
      const int off = WS_G3 + (((vq * 7 + w) * 4 + g) * 56 + f) * 128 + (n & 3) * 16;
      __builtin_amdgcn_raw_buffer_store_b128(gran2(a[0], a[1], tag), rs, off, 0, SC1);
      __builtin_amdgcn_raw_buffer_store_b128(gran2(a[2], a[3], tag), rs, off + 64, 0, SC1);
    }
    STAMP(10);
  }
  STAMP_FLUSH();
}

extern "C" void kernel_launch(void* const* d_in, const int* in_sizes, int n_in, void* d_out, int out_size, void* d_ws, size_t ws_size,
                              hipStream_t stream) {
  Params p;
  p.x = (const u16*)d_in[0]; p.kc = (const u16*)d_in[1]; p.vc = (const u16*)d_in[2];
  p.wq = (const u16*)d_in[3]; p.wo = (const u16*)d_in[4];
  p.wg = (const u16*)d_in[6]; p.wu = (const u16*)d_in[7]; p.wd = (const u16*)d_in[8];
  p.out = (u16*)d_out; p.ws = (unsigned char*)d_ws;
  fused_decode_block<<<256, 512, 0, stream>>>(p);
}
```

```cpp
#define PROBE_K 0
#define PROBE_A 0
#define PROBE_B 1
#include <hip/hip_runtime.h>
#include <hip/hip_bf16.h>

typedef unsigned short u16;
typedef unsigned long long u64;
typedef unsigned v4u __attribute__((ext_vector_type(4)));
typedef unsigned v2u __attribute__((ext_vector_type(2)));
typedef float f32x4 __attribute__((ext_vector_type(4)));
typedef float f32x2 __attribute__((ext_vector_type(2)));
typedef short s16x4 __attribute__((ext_vector_type(4)));
typedef __bf16 bf16x8 __attribute__((ext_vector_type(8)));
typedef __bf16 bf16x2 __attribute__((ext_vector_type(2)));
#define GAS __attribute__((address_space(1)))
#define LAS __attribute__((address_space(3)))

constexpr int D = 448, SK = 4096, WIN = 1024, FF = 1792;
constexpr int WS_EPOCH = 0;
constexpr int WS_GQ = 4096;
constexpr int WS_GS = WS_GQ + 8 * 4 * 4 * 512 * 8;
constexpr int WS_GC = WS_GS + 8 * 32 * 4 * 8;
constexpr int WS_G1 = WS_GC + 8 * 8 * 32 * 64 * 8;
constexpr int WS_G2 = WS_G1 + 112 * 64 * 16 * 8;
constexpr int WS_G3 = WS_G2 + 8192;
constexpr int WS_END = WS_G3 + 112 * 56 * 16 * 8;
constexpr int WS_STAMP = 8 << 20;
constexpr int L_K = 0, L_V = 65536, L_WQ = 131072, L_XN = L_WQ + 14336, XN_STR = 912;
constexpr int L_Q = L_XN + 3712, Q_STR = 576;
constexpr int L_SC = L_Q + 1152;
constexpr int L_P = L_SC + 512, P_STR = 320;
constexpr int L_OB = L_P + 8 * 640, OB_STR = 192;
constexpr int L_ACT = L_OB + 768;
constexpr int L_END = L_ACT + 256;
static_assert(L_END <= 163840, "LDS");
constexpr unsigned SPIN_CAP = 1u << 16;
constexpr int SC1 = 16;
constexpr float LOG2E = 1.4426950408889634f;

struct Params {
  const u16 *x, *kc, *vc, *wq, *wo, *wg, *wu, *wd;
  u16* out;
  unsigned char* ws;
};

#define SB() __builtin_amdgcn_sched_barrier(0)
#define MEMFENCE() asm volatile("" ::: "memory")
#define WG_BARRIER() asm volatile("s_waitcnt lgkmcnt(0)\n\ts_barrier" ::: "memory")
#define LDS_WAIT() asm volatile("s_waitcnt lgkmcnt(0)" ::: "memory")

#define STAMP(i) do { SB(); ts[i] = (unsigned)__builtin_amdgcn_s_memrealtime(); SB(); } while (0)
#define STAMP_DECL do {} while (0)
#define STAMP_FLUSH() do { if (blockIdx.x == 128 && (threadIdx.x >> 6) == 7) { const unsigned dl_ = PROBE_K * (ts[PROBE_B] - ts[PROBE_A]); while ((unsigned)__builtin_amdgcn_s_memrealtime() - ts[11] < dl_) {} } } while (0)

__device__ __forceinline__ v4u ldnt(const void* q) { return __builtin_nontemporal_load((const GAS v4u*)q); }
__device__ __forceinline__ float bflo(unsigned u) { return __builtin_bit_cast(float, u << 16); }
__device__ __forceinline__ float bfhi(unsigned u) { return __builtin_bit_cast(float, u & 0xffff0000u); }
__device__ __forceinline__ unsigned pk(float lo, float hi) {
  f32x2 v = {lo, hi};
  return __builtin_bit_cast(unsigned, __builtin_convertvector(v, bf16x2));
}
__device__ __forceinline__ float rbf(float f) { return bflo(pk(f, f)); }
template <int CTRL> __device__ __forceinline__ float dpp(float x) {
  return __builtin_bit_cast(float, __builtin_amdgcn_mov_dpp(__builtin_bit_cast(int, x), CTRL, 0xf, 0xf, true));
}
constexpr int ROR1 = 0x121, ROR2 = 0x122, ROR4 = 0x124, ROR8 = 0x128;
__device__ __forceinline__ float x16_sum(float x) {
  auto s = __builtin_amdgcn_permlane16_swap(__float_as_uint(x), __float_as_uint(x), false, false);
  return __uint_as_float(s[0]) + __uint_as_float(s[1]);
}
__device__ __forceinline__ float x32_sum(float x) {
  auto s = __builtin_amdgcn_permlane32_swap(__float_as_uint(x), __float_as_uint(x), false, false);
  return __uint_as_float(s[0]) + __uint_as_float(s[1]);
}
__device__ __forceinline__ float x16_max(float x) {
  auto s = __builtin_amdgcn_permlane16_swap(__float_as_uint(x), __float_as_uint(x), false, false);
  return fmaxf(__uint_as_float(s[0]), __uint_as_float(s[1]));
}
__device__ __forceinline__ float x32_max(float x) {
  auto s = __builtin_amdgcn_permlane32_swap(__float_as_uint(x), __float_as_uint(x), false, false);
  return fmaxf(__uint_as_float(s[0]), __uint_as_float(s[1]));
}
__device__ __forceinline__ float wave_sum(float x) {
  x += dpp<ROR8>(x); x += dpp<ROR4>(x); x += dpp<ROR2>(x); x += dpp<ROR1>(x);
  return x32_sum(x16_sum(x));
}
__device__ __forceinline__ float wave_max(float x) {
  x = fmaxf(x, dpp<ROR8>(x)); x = fmaxf(x, dpp<ROR4>(x)); x = fmaxf(x, dpp<ROR2>(x)); x = fmaxf(x, dpp<ROR1>(x));
  return x32_max(x16_max(x));
}
__device__ __forceinline__ f32x4 mfma32(v4u a, v4u b, f32x4 c) {
  return __builtin_amdgcn_mfma_f32_16x16x32_bf16(__builtin_bit_cast(bf16x8, a), __builtin_bit_cast(bf16x8, b), c, 0, 0, 0);
}
__device__ __forceinline__ f32x4 mfma16(v2u a, v2u b, f32x4 c) {
  return __builtin_amdgcn_mfma_f32_16x16x16bf16_1k(__builtin_bit_cast(s16x4, a), __builtin_bit_cast(s16x4, b), c, 0, 0, 0);
}
__device__ __forceinline__ v2u trd(LAS unsigned char* a) {
  return __builtin_bit_cast(v2u, __builtin_amdgcn_ds_read_tr16_b64_v4i16((LAS s16x4*)a));
}
__device__ __forceinline__ v4u trd2(LAS unsigned char* a0, LAS unsigned char* a1) {
  const v2u lo = trd(a0), hi = trd(a1);
  return v4u{lo.x, lo.y, hi.x, hi.y};
}
__device__ __forceinline__ v4u layernorm_row(v4u v, bool act) {
  float f[8];
#pragma unroll
  for (int i = 0; i < 4; ++i) { f[2 * i] = bflo(v[i]); f[2 * i + 1] = bfhi(v[i]); }
  const float x0 = __builtin_bit_cast(float, __builtin_amdgcn_readfirstlane(__builtin_bit_cast(int, f[0])));
  float s1 = 0.f, s2 = 0.f;
#pragma unroll
  for (int i = 0; i < 8; ++i) { const float d = f[i] - x0; s1 += d; s2 = fmaf(d, d, s2); }
  s1 = act ? s1 : 0.f; s2 = act ? s2 : 0.f;
  s1 = wave_sum(s1); s2 = wave_sum(s2);
  const float md = s1 * (1.0f / D);
  const float var = fmaxf(fmaf(-md, md, s2 * (1.0f / D)), 0.f);
  const float m = rbf(x0 + md);
  const float den = rbf(__builtin_amdgcn_sqrtf(rbf(rbf(var) + 1e-5f)));
  const float inv = __builtin_amdgcn_rcpf(den);
  v4u o;
#pragma unroll
  for (int i = 0; i < 4; ++i) {
    const unsigned d = pk(f[2 * i] - m, f[2 * i + 1] - m);
    o[i] = pk(bflo(d) * inv, bfhi(d) * inv);
  }
  return o;
}
__device__ __forceinline__ f32x4 sel4(int sub, f32x4 a0, f32x4 a1, f32x4 a2, f32x4 a3) {
  const f32x4 lo = (sub & 1) ? a1 : a0, hi = (sub & 1) ? a3 : a2;
  return (sub & 2) ? hi : lo;
}
__device__ __forceinline__ v4u gran2(float a, float b, unsigned tag) { return v4u{__float_as_uint(a), tag, __float_as_uint(b), tag}; }

__global__ void __launch_bounds__(512, 2) fused_decode_block(Params p) {
  unsigned ts[16] = {0,0,0,0,0,0,0,0,0,0,0,0,0,0,0,0};
  { u64 t_; asm volatile("s_memrealtime %0\n\ts_waitcnt lgkmcnt(0)" : "=s"(t_) :: "memory"); ts[15] = (unsigned)t_; SB(); asm volatile("" :: "s"(p.x), "s"(p.ws) : "memory"); SB(); }
  __shared__ __attribute__((aligned(16))) unsigned char smem[L_END];
  LAS unsigned char* L = (LAS unsigned char*)smem;
  const int c = blockIdx.x, tid = threadIdx.x, lane = tid & 63, w = __builtin_amdgcn_readfirstlane(tid >> 6);
  const int kvh = c & 7, j = c >> 3, b = j >> 3, s = j & 7;
  const int n = lane & 15, g = lane >> 4;
  const __amdgpu_buffer_rsrc_t rs = __builtin_amdgcn_make_buffer_rsrc((void*)p.ws, 0, WS_END, 0x00020000);
  STAMP_DECL;
  STAMP(0);

  const unsigned ep = __builtin_amdgcn_raw_buffer_load_b32(rs, WS_EPOCH, 0, SC1);
  const v4u xr = *(const GAS v4u*)(p.x + (w & 3) * D + (lane < 56 ? lane : 55) * 8);
  const int w7 = w < 7 ? w : 6;
  const char* wqp = (const char*)p.wq + (size_t)(b * 112 + w7 * 16 + (lane >> 3)) * 8192 + (kvh * 512 + s * 64) * 2 + (lane & 7) * 16;
  const v4u wq0 = ldnt(wqp), wq1 = ldnt(wqp + 8 * 8192);
  SB();
  const size_t kvoff = ((size_t)(b * 8 + kvh) * SK + (SK - WIN)) * 512 + (size_t)(s + 8 * w) * 1024 + lane * 16;
  const char* kp = (const char*)p.kc + kvoff;
  const char* vp = (const char*)p.vc + kvoff;
  v4u K[8], V[8];
#pragma unroll
  for (int i = 0; i < 4; ++i) K[i] = ldnt(kp + i * 65536);
  SB();
  const unsigned tag = __builtin_amdgcn_readfirstlane(ep) + 1u;

  if (w >= 4) {
    const v4u xn = layernorm_row(xr, lane < 56);
    if (lane < 56) *(LAS v4u*)(L + L_XN + (w & 3) * XN_STR + lane * 16) = xn;
  }
  if (w < 7) {
    const int r0 = w * 16 + (lane >> 3), ch = lane & 7;
    *(LAS v4u*)(L + L_WQ + r0 * 128 + ((ch ^ (((r0 >> 1) & 3) << 1)) << 4)) = wq0;
    *(LAS v4u*)(L + L_WQ + (r0 + 8) * 128 + ((ch ^ (((r0 >> 1) & 3) << 1)) << 4)) = wq1;
  }
  STAMP(1);
  WG_BARRIER();
  if (w < 2) {
    f32x4 a0 = {0.f, 0.f, 0.f, 0.f}, a1 = a0;
    const int row = 4 * g + (n >> 2), sw = (row >> 1) & 3;
    LAS unsigned char* t0 = L + L_WQ + row * 128 + (((2 * w) ^ sw) << 5) + (n & 3) * 8;
    LAS unsigned char* t1 = L + L_WQ + row * 128 + (((2 * w + 1) ^ sw) << 5) + (n & 3) * 8;
    LAS unsigned char* xb = L + L_XN + (n >> 2) * XN_STR + (b * 112 + 4 * g) * 2;
#pragma unroll
    for (int ks = 0; ks < 7; ++ks) {
      const v2u xf = *(LAS v2u*)(xb + ks * 32);
      a0 = mfma16(trd(t0 + ks * 2048), xf, a0);
      a1 = mfma16(trd(t1 + ks * 2048), xf, a1);
    }
    const f32x4 a = (n & 2) ? a1 : a0;
    const float e0 = (n & 1) ? a[2] : a[0], e1 = (n & 1) ? a[3] : a[1];
    const int off = WS_GQ + (((kvh * 4 + (n >> 2)) * 4 + b) * 256 + (s * 2 + w) * 16 + g * 4 + (n & 3)) * 16;
    __builtin_amdgcn_raw_buffer_store_b128(gran2(e0, e1, tag), rs, off, 0, SC1);
  }
  STAMP(2);

  LAS unsigned char* kw = L + L_K + w * 8192;
  const int kch = lane & 31, khalf = lane >> 5;
#pragma unroll
  for (int i = 0; i < 4; ++i) {
    const int key = 2 * i + khalf;
    *(LAS v4u*)(kw + key * 512 + (((kch & 16) | ((kch ^ key) & 15)) << 4)) = K[i];
    SB();
    K[4 + i] = ldnt(kp + (4 + i) * 65536);
    SB();
  }
  const int gqoff = WS_GQ + ((kvh * 4 + b) * 4) * 4096 + tid * 8;
  v2u qg[4];
#pragma unroll
  for (int i = 4; i < 8; ++i) {
    const int key = 2 * i + khalf;
    *(LAS v4u*)(kw + key * 512 + (((kch & 16) | ((kch ^ key) & 15)) << 4)) = K[i];
    SB();
    V[i - 4] = ldnt(vp + (i - 4) * 65536);
    SB();
    if (i == 4) {
#pragma unroll
      for (int k = 0; k < 4; ++k) qg[k] = __builtin_amdgcn_raw_buffer_load_b64(rs, gqoff + k * 4096, 0, SC1);
      SB();
    }
  }
  STAMP(3);
  float qv = (__uint_as_float(qg[0].x) + __uint_as_float(qg[1].x)) + (__uint_as_float(qg[2].x) + __uint_as_float(qg[3].x));
  if (__builtin_expect(!__all((qg[0].y == tag) & (qg[1].y == tag) & (qg[2].y == tag) & (qg[3].y == tag)), 0)) {
    for (unsigned spins = 0;; ) {
      MEMFENCE();
#pragma unroll
      for (int k = 0; k < 4; ++k) qg[k] = __builtin_amdgcn_raw_buffer_load_b64(rs, gqoff + k * 4096, 0, SC1);
      qv = (__uint_as_float(qg[0].x) + __uint_as_float(qg[1].x)) + (__uint_as_float(qg[2].x) + __uint_as_float(qg[3].x));
      const bool ok = (qg[0].y == tag) & (qg[1].y == tag) & (qg[2].y == tag) & (qg[3].y == tag);
      if (__all(ok) || ++spins > SPIN_CAP) break;
    }
  }
  {
    const int pos = tid >> 1, sub = pos & 3, qq = (pos >> 2) & 3, tp = pos >> 4;
    const int col = 32 * tp + 16 * (sub >> 1) + 4 * qq + 2 * (sub & 1) + (tid & 1);
    *(LAS u16*)(L + L_Q + (col >> 8) * Q_STR + (col & 255) * 2) = (u16)pk(qv, qv);
  }
  SB();
  V[4] = ldnt(vp + 4 * 65536);
  SB();
  STAMP(4);
  WG_BARRIER();
  {
    f32x4 a = {0.f, 0.f, 0.f, 0.f};
#pragma unroll
    for (int t = 0; t < 8; ++t) {
      const int ch = 4 * t + g;
      const v4u kf = *(LAS v4u*)(kw + n * 512 + (((ch & 16) | ((ch ^ n) & 15)) << 4));
      const v4u qf = *(LAS v4u*)(L + L_Q + (n & 1) * Q_STR + ch * 16);
      a = mfma32(qf, kf, a);
    }
    if (lane < 16) *(LAS unsigned*)(L + L_SC + (16 * (n >> 1) + 2 * w + (n & 1)) * 4) = pk(a[0], a[1]);
  }
  WG_BARRIER();
  const int vch = lane & 31;
#define STAGE_V(i) do { const int row_ = 16 * (i) + 2 * w + khalf; \
    *(LAS v4u*)(L + L_V + row_ * 512 + ((vch ^ ((((row_ & 3) << 1) | ((row_ >> 3) & 1)) << 1)) << 4)) = V[i]; } while (0)
  STAGE_V(0); STAGE_V(1);
  SB();
  V[5] = ldnt(vp + 5 * 65536);
  SB();
  LAS unsigned char* pw = L + L_P + w * 640;
  {
    const v2u sc = *(LAS v2u*)(L + L_SC + lane * 8);
    const float s00 = bflo(sc.x) * 0.0625f, s10 = bfhi(sc.x) * 0.0625f, s01 = bflo(sc.y) * 0.0625f, s11 = bfhi(sc.y) * 0.0625f;
    const float m0 = wave_max(fmaxf(s00, s01)), m1 = wave_max(fmaxf(s10, s11));
    const float e00 = __builtin_amdgcn_exp2f((s00 - m0) * LOG2E), e01 = __builtin_amdgcn_exp2f((s01 - m0) * LOG2E);
    const float e10 = __builtin_amdgcn_exp2f((s10 - m1) * LOG2E), e11 = __builtin_amdgcn_exp2f((s11 - m1) * LOG2E);
    *(LAS unsigned*)(pw + lane * 4) = pk(e00, e01);
    *(LAS unsigned*)(pw + P_STR + lane * 4) = pk(e10, e11);
    if (w == 7) {
      const float l0 = wave_sum(e00 + e01), l1 = wave_sum(e10 + e11);
      if (lane < 2) __builtin_amdgcn_raw_buffer_store_b128(gran2(lane ? m1 : m0, lane ? l1 : l0, tag), rs, WS_GS + (kvh * 32 + j) * 32 + lane * 16, 0, SC1);
    }
  }
  STAMP(5);
  STAGE_V(2);
  SB();
  V[6] = ldnt(vp + 6 * 65536);
  SB();
  STAGE_V(3);
  SB();
  V[7] = ldnt(vp + 7 * 65536);
  SB();
  STAGE_V(4); STAGE_V(5);
  v4u pf[4];
#pragma unroll
  for (int t = 0; t < 4; ++t) pf[t] = *(LAS v4u*)(pw + (n & 1) * P_STR + t * 64 + g * 16);
  WG_BARRIER();
  f32x4 o0 = {0.f, 0.f, 0.f, 0.f}, o1 = o0;
  const int vq = n >> 2, vsw = (vq << 1) | (g & 1);
  LAS unsigned char* v0 = L + L_V + (8 * g + vq) * 512 + (((2 * w) ^ vsw) << 5) + (n & 3) * 8;
  LAS unsigned char* v1 = L + L_V + (8 * g + vq) * 512 + (((2 * w + 1) ^ vsw) << 5) + (n & 3) * 8;
#pragma unroll
  for (int t = 0; t < 3; ++t) {
    o0 = mfma32(trd2(v0 + t * 16384, v0 + t * 16384 + 2048), pf[t], o0);
    o1 = mfma32(trd2(v1 + t * 16384, v1 + t * 16384 + 2048), pf[t], o1);
  }
  SB();
  STAGE_V(6); STAGE_V(7);
  STAMP(6);
  WG_BARRIER();
  o0 = mfma32(trd2(v0 + 3 * 16384, v0 + 3 * 16384 + 2048), pf[3], o0);
  o1 = mfma32(trd2(v1 + 3 * 16384, v1 + 3 * 16384 + 2048), pf[3], o1);
  {
    const f32x4 a = (n & 2) ? o1 : o0;
    const float e0 = (n & 4) ? a[2] : a[0], e1 = (n & 4) ? a[3] : a[1];
    if (n < 8) __builtin_amdgcn_raw_buffer_store_b128(gran2(e0, e1, tag), rs, WS_GC + ((kvh * 8 + w) * 32 + j) * 512 + (g * 8 + n) * 16, 0, SC1);
  }
  STAMP(7);

  const int lrow = lane >> 3, lch = lane & 7;
  const int tsw = ((vq >> 1) & 1) | ((g & 1) << 1);
#define LT_WADDR(base, row) ((base) + (row) * 128 + ((lch ^ (((((row) >> 1) & 1) | ((((row) >> 3) & 1) << 1)) << 1)) << 4))
#define LT_RADDR(base, ks, t, h) ((base) + (32 * (ks) + 8 * g + 4 * (h) + vq) * 128 + (((t) ^ tsw) << 5) + (n & 3) * 8)

  if (j >= 8) {
    const int rsl = j & 7, cg = (j >> 3) - 1;
    const int nline = cg == 0 ? 3 : 2, line0 = cg == 0 ? 0 : 2 * cg + 1;
    v4u wf[2][4];
    if (w >= 4 && w - 4 < nline) {
      const char* wop = (const char*)p.wo + (size_t)(kvh * 512 + rsl * 32 + lrow) * 896 + (line0 + w - 4) * 128 + lch * 16;
      v4u t[8];
#pragma unroll
      for (int i = 0; i < 8; ++i) t[i] = ldnt(wop + ((i & 3) * 8 + (i >> 2) * 256) * 896);
      SB();
#pragma unroll
      for (int i = 0; i < 8; ++i) *(LAS v4u*)LT_WADDR(kw, 8 * i + lrow) = t[i];
      LDS_WAIT();
#pragma unroll
      for (int ks = 0; ks < 2; ++ks)
#pragma unroll
        for (int t4 = 0; t4 < 4; ++t4) wf[ks][t4] = trd2(LT_RADDR(kw, ks, t4, 0), LT_RADDR(kw, ks, t4, 1));
    } else {
#pragma unroll
      for (int ks = 0; ks < 2; ++ks)
#pragma unroll
        for (int t4 = 0; t4 < 4; ++t4) wf[ks][t4] = v4u{0u, 0u, 0u, 0u};
    }
    if (w < 4) {
      v4u st;
      for (unsigned spins = 0;; ) {
        MEMFENCE();
        st = __builtin_amdgcn_raw_buffer_load_b128(rs, WS_GS + kvh * 1024 + lane * 16, 0, SC1);
        if (__all((st.y == tag) & (st.w == tag)) || ++spins > SPIN_CAP) break;
      }
      const float m = __uint_as_float(st.x), l = __uint_as_float(st.z);
      float M = fmaxf(m, dpp<ROR2>(m)); M = fmaxf(M, dpp<ROR4>(M)); M = fmaxf(M, dpp<ROR8>(M));
      const float e = __builtin_amdgcn_exp2f((m - M) * LOG2E);
      float Ls = l * e; Ls += dpp<ROR2>(Ls); Ls += dpp<ROR4>(Ls); Ls += dpp<ROR8>(Ls);
      const float wt = e * __builtin_amdgcn_rcpf(Ls);
      float cw[4];
#pragma unroll
      for (int u = 0; u < 4; ++u) cw[u] = __shfl(wt, 16 * w + 4 * u + 2 * khalf + (lane & 1));
      v4u v[4];
      const int off = WS_GC + ((kvh * 8 + rsl) * 32 + 8 * w) * 512 + lane * 16;
      for (unsigned spins = 0;; ) {
        MEMFENCE();
        bool ok = true;
#pragma unroll
        for (int u = 0; u < 4; ++u) v[u] = __builtin_amdgcn_raw_buffer_load_b128(rs, off + u * 1024, 0, SC1);
#pragma unroll
        for (int u = 0; u < 4; ++u) ok &= (v[u].y == tag) & (v[u].w == tag);
        if (__all(ok) || ++spins > SPIN_CAP) break;
      }
      float a0 = 0.f, a1 = 0.f;
#pragma unroll
      for (int u = 0; u < 4; ++u) { a0 = fmaf(cw[u], __uint_as_float(v[u].x), a0); a1 = fmaf(cw[u], __uint_as_float(v[u].z), a1); }
      a0 = x32_sum(a0); a1 = x32_sum(a1);
      const int pn = lane & 7, pq = (lane >> 3) & 3;
      const int kap = (pn & 1) * 32 + 16 * ((pn >> 1) & 1) + 4 * pq + 2 * (pn >> 2);
      if (lane < 32) *(LAS unsigned*)(L + L_OB + w * OB_STR + kap * 2) = pk(a0, a1);
    }
    STAMP(8);
    WG_BARRIER();
    if (w >= 4 && w - 4 < nline) {
      const v4u b0 = *(LAS v4u*)(L + L_OB + vq * OB_STR + g * 16), b1 = *(LAS v4u*)(L + L_OB + vq * OB_STR + 64 + g * 16);
      f32x4 acc[4];
#pragma unroll
      for (int t4 = 0; t4 < 4; ++t4) acc[t4] = mfma32(wf[0][t4], b0, f32x4{0.f, 0.f, 0.f, 0.f});
#pragma unroll
      for (int t4 = 0; t4 < 4; ++t4) acc[t4] = mfma32(wf[1][t4], b1, acc[t4]);
      const f32x4 a = sel4(n & 3, acc[0], acc[1], acc[2], acc[3]);
      const int off = WS_G1 + (((vq * 7 + line0 + w - 4) * 4 + g) * 64 + kvh * 8 + rsl) * 128 + (n & 3) * 16;
      __builtin_amdgcn_raw_buffer_store_b128(gran2(a[0], a[1], tag), rs, off, 0, SC1);
      __builtin_amdgcn_raw_buffer_store_b128(gran2(a[2], a[3], tag), rs, off + 64, 0, SC1);
    }
    STAMP(9);
    if (w == 7 && j >= 16 && j < 30) {
      const int r = (j - 16) * 8 + kvh;
      const int rq = r & 3, rl = (r >> 2) % 7, rb = r / 28;
      const int col = 64 * rl + 16 * (lane & 3) + 4 * rq + 2 * ((lane >> 2) & 1);
      const unsigned xin = *(const GAS unsigned*)(p.x + rb * D + col);
      float a0, a1;
      for (unsigned spins = 0;; ) {
        MEMFENCE();
        v4u v[8]; bool ok = true;
#pragma unroll
        for (int i = 0; i < 8; ++i) v[i] = __builtin_amdgcn_raw_buffer_load_b128(rs, WS_G1 + r * 8192 + i * 1024 + lane * 16, 0, SC1);
        a0 = 0.f; a1 = 0.f;
#pragma unroll
        for (int i = 0; i < 8; ++i) { ok &= (v[i].y == tag) & (v[i].w == tag); a0 += __uint_as_float(v[i].x); a1 += __uint_as_float(v[i].z); }
        if (__all(ok) || ++spins > SPIN_CAP) break;
      }
      a0 += dpp<ROR8>(a0); a1 += dpp<ROR8>(a1);
      a0 = x32_sum(x16_sum(a0)); a1 = x32_sum(x16_sum(a1));
      const unsigned at = pk(a0, a1);
      const unsigned h = pk(bflo(xin) + bflo(at), bfhi(xin) + bfhi(at));
      if (lane < 8) __builtin_amdgcn_raw_buffer_store_b64(v2u{h, tag}, rs, WS_G2 + r * 64 + lane * 8, 0, SC1);
      if (r == 0 && lane == 0) __builtin_amdgcn_raw_buffer_store_b32(tag, rs, WS_EPOCH, 0, SC1);
      STAMP(10);
      for (unsigned spins = 0;; ) {
        MEMFENCE();
        v4u v[7]; bool ok = true;
#pragma unroll
        for (int i = 0; i < 7; ++i) v[i] = __builtin_amdgcn_raw_buffer_load_b128(rs, WS_G3 + r * 7168 + i * 1024 + lane * 16, 0, SC1);
        a0 = 0.f; a1 = 0.f;
#pragma unroll
        for (int i = 0; i < 7; ++i) { ok &= (v[i].y == tag) & (v[i].w == tag); a0 += __uint_as_float(v[i].x); a1 += __uint_as_float(v[i].z); }
        if (__all(ok) || ++spins > SPIN_CAP) break;
      }
      a0 += dpp<ROR8>(a0); a1 += dpp<ROR8>(a1);
      a0 = x32_sum(x16_sum(a0)); a1 = x32_sum(x16_sum(a1));
      const unsigned ff = pk(a0, a1);
      if (lane < 8) *(GAS unsigned*)(p.out + rb * D + col) = pk(bflo(h) + bflo(ff), bfhi(h) + bfhi(ff));
      STAMP(11);
    }
  } else if (j < 7) {
    const int f = kvh * 7 + j;
    v4u df[4];
    WG_BARRIER();
    if (w < 7) {
      const size_t go = (size_t)(w * 64 + (lane >> 2)) * (FF * 2) + f * 64 + (lane & 3) * 16;
      const char* wdp = (const char*)p.wd + (size_t)(f * 32 + lrow) * 896 + w * 128 + lch * 16;
      v4u tg[4], tu[4], td[4];
#pragma unroll
      for (int i = 0; i < 4; ++i) { tg[i] = ldnt((const char*)p.wg + go + (size_t)i * 16 * FF * 2); tu[i] = ldnt((const char*)p.wu + go + (size_t)i * 16 * FF * 2); }
#pragma unroll
      for (int i = 0; i < 4; ++i) td[i] = ldnt(wdp + i * 8 * 896);
      SB();
#pragma unroll
      for (int i = 0; i < 4; ++i) {
        const int row = w * 64 + i * 16 + (lane >> 2), m = ((row >> 1) & 1) | (((row >> 3) & 1) << 1);
        *(LAS v4u*)(L + L_K + row * 128 + (((lane & 3) ^ m) << 4)) = tg[i];
        *(LAS v4u*)(L + L_K + row * 128 + 64 + (((lane & 3) ^ m) << 4)) = tu[i];
      }
      LAS unsigned char* dw = L + L_V + w * 4096;
#pragma unroll
      for (int i = 0; i < 4; ++i) *(LAS v4u*)LT_WADDR(dw, 8 * i + lrow) = td[i];
      LDS_WAIT();
#pragma unroll
      for (int t4 = 0; t4 < 4; ++t4) df[t4] = trd2(LT_RADDR(dw, 0, t4, 0), LT_RADDR(dw, 0, t4, 1));
    } else {
#pragma unroll
      for (int t4 = 0; t4 < 4; ++t4) df[t4] = v4u{0u, 0u, 0u, 0u};
    }
    WG_BARRIER();
    STAMP(8);
    v4u gf[14];
    if (w < 4) {
      LAS unsigned char* gb = L + L_K + (8 * g + vq) * 128 + ((n & 2) << 5) + ((w ^ tsw) << 4) + (n & 1) * 8;
#pragma unroll
      for (int ks = 0; ks < 14; ++ks) gf[ks] = trd2(gb + ks * 4096, gb + ks * 4096 + 512);
    } else {
#pragma unroll
      for (int ks = 0; ks < 14; ++ks) gf[ks] = v4u{0u, 0u, 0u, 0u};
      const int l56 = lane < 56 ? lane : 55;
      v4u h0, h1;
      for (unsigned spins = 0;; ) {
        MEMFENCE();
        h0 = __builtin_amdgcn_raw_buffer_load_b128(rs, WS_G2 + (w - 4) * 1792 + l56 * 16, 0, SC1);
        h1 = __builtin_amdgcn_raw_buffer_load_b128(rs, WS_G2 + (w - 4) * 1792 + 896 + l56 * 16, 0, SC1);
        if (__all((h0.y == tag) & (h0.w == tag) & (h1.y == tag) & (h1.w == tag)) || ++spins > SPIN_CAP) break;
      }
      STAMP(9);
      const v4u hn = layernorm_row(v4u{h0.x, h0.z, h1.x, h1.z}, lane < 56);
      if (lane < 56) {
        LAS unsigned char* hb = L + L_XN + (w - 4) * XN_STR;
        const int p0 = lane, p1 = lane + 56;
        const int c0 = 64 * (p0 >> 4) + 4 * ((p0 >> 2) & 3) + 2 * ((p0 >> 1) & 1) + 32 * (p0 & 1);
        const int c1 = 64 * (p1 >> 4) + 4 * ((p1 >> 2) & 3) + 2 * ((p1 >> 1) & 1) + 32 * (p1 & 1);
        *(LAS unsigned*)(hb + c0 * 2) = hn.x; *(LAS unsigned*)(hb + c0 * 2 + 32) = hn.y;
        *(LAS unsigned*)(hb + c1 * 2) = hn.z; *(LAS unsigned*)(hb + c1 * 2 + 32) = hn.w;
      }
    }
    WG_BARRIER();
    if (w < 4) {
      f32x4 a = {0.f, 0.f, 0.f, 0.f};
      LAS unsigned char* hb = L + L_XN + vq * XN_STR + g * 16;
#pragma unroll
      for (int ks = 0; ks < 14; ++ks) a = mfma32(gf[ks], *(LAS v4u*)(hb + ks * 64), a);
      const float lo = (n & 1) ? a[1] : a[0], hi = (n & 1) ? a[3] : a[2];
      const float mine = rbf((n & 2) ? hi : lo);
      auto sw = __builtin_amdgcn_permlane32_swap(__float_as_uint(mine), __float_as_uint(mine), false, false);
      const float gt = __uint_as_float(sw[0]), up = __uint_as_float(sw[1]);
      const float sg = rbf(gt * __builtin_amdgcn_rcpf(1.0f + __builtin_amdgcn_exp2f(-gt * LOG2E)));
      const float av = sg * up;
      if (lane < 32) *(LAS u16*)(L + L_ACT + vq * 64 + (8 * w + 4 * g + (n & 3)) * 2) = (u16)pk(av, av);
    }
    WG_BARRIER();
    if (w < 7) {
      const v4u af = *(LAS v4u*)(L + L_ACT + vq * 64 + g * 16);
      f32x4 acc[4];
#pragma unroll
      for (int t4 = 0; t4 < 4; ++t4) acc[t4] = mfma32(df[t4], af, f32x4{0.f, 0.f, 0.f, 0.f});
      const f32x4 a = sel4(n & 3, acc[0], acc[1], acc[2], acc[3]);
      const int off = WS_G3 + (((vq * 7 + w) * 4 + g) * 56 + f) * 128 + (n & 3) * 16;
      __builtin_amdgcn_raw_buffer_store_b128(gran2(a[0], a[1], tag), rs, off, 0, SC1);
      __builtin_amdgcn_raw_buffer_store_b128(gran2(a[2], a[3], tag), rs, off + 64, 0, SC1);
    }
    STAMP(10);
  }
  STAMP_FLUSH();
}

extern "C" void kernel_launch(void* const* d_in, const int* in_sizes, int n_in, void* d_out, int out_size, void* d_ws, size_t ws_size,
                              hipStream_t stream) {
  Params p;
  p.x = (const u16*)d_in[0]; p.kc = (const u16*)d_in[1]; p.vc = (const u16*)d_in[2];
  p.wq = (const u16*)d_in[3]; p.wo = (const u16*)d_in[4];
  p.wg = (const u16*)d_in[6]; p.wu = (const u16*)d_in[7]; p.wd = (const u16*)d_in[8];
  p.out = (u16*)d_out; p.ws = (unsigned char*)d_ws;
  fused_decode_block<<<256, 512, 0, stream>>>(p);
}
```

```cpp
#include <hip/hip_runtime.h>
#include <hip/hip_bf16.h>

typedef unsigned short u16;
typedef unsigned long long u64;
typedef unsigned v4u __attribute__((ext_vector_type(4)));
typedef unsigned v2u __attribute__((ext_vector_type(2)));
typedef float f32x4 __attribute__((ext_vector_type(4)));
typedef float f32x2 __attribute__((ext_vector_type(2)));
typedef short s16x4 __attribute__((ext_vector_type(4)));
typedef __bf16 bf16x8 __attribute__((ext_vector_type(8)));
typedef __bf16 bf16x2 __attribute__((ext_vector_type(2)));
#define GAS __attribute__((address_space(1)))
#define LAS __attribute__((address_space(3)))

constexpr int D = 448, SK = 4096, WIN = 1024, FF = 1792;
constexpr int WS_EPOCH = 0;
constexpr int WS_GQ = 4096;
constexpr int WS_GS = WS_GQ + 8 * 4 * 4 * 512 * 8;
constexpr int WS_GC = WS_GS + 8 * 32 * 4 * 8;
constexpr int WS_G1 = WS_GC + 8 * 8 * 32 * 64 * 8;
constexpr int WS_G2 = WS_G1 + 112 * 64 * 16 * 8;
constexpr int WS_G3 = WS_G2 + 8192;
constexpr int WS_END = WS_G3 + 112 * 56 * 16 * 8;
constexpr int WS_STAMP = 8 << 20;
constexpr int L_K = 0, L_V = 65536, L_WQ = 131072, L_XN = L_WQ + 14336, XN_STR = 912;
constexpr int L_Q = L_XN + 3712, Q_STR = 576;
constexpr int L_SC = L_Q + 1152;
constexpr int L_P = L_SC + 512, P_STR = 320;
constexpr int L_OB = L_P + 8 * 640, OB_STR = 192;
constexpr int L_ACT = L_OB + 768;
constexpr int L_END = L_ACT + 256;
static_assert(L_END <= 163840, "LDS");
constexpr unsigned SPIN_CAP = 1u << 16;
constexpr int SC1 = 16;
constexpr float LOG2E = 1.4426950408889634f;

struct Params {
  const u16 *x, *kc, *vc, *wq, *wo, *wg, *wu, *wd;
  u16* out;
  unsigned char* ws;
};

#define SB() __builtin_amdgcn_sched_barrier(0)
#define MEMFENCE() asm volatile("" ::: "memory")
#define WG_BARRIER() asm volatile("s_waitcnt lgkmcnt(0)\n\ts_barrier" ::: "memory")
#define LDS_WAIT() asm volatile("s_waitcnt lgkmcnt(0)" ::: "memory")

#define STAMP(i) do {} while (0)
#define STAMP_DECL do {} while (0)
#define STAMP_FLUSH() do {} while (0)

__device__ __forceinline__ v4u ldnt(const void* q) { return __builtin_nontemporal_load((const GAS v4u*)q); }
__device__ __forceinline__ float bflo(unsigned u) { return __builtin_bit_cast(float, u << 16); }
__device__ __forceinline__ float bfhi(unsigned u) { return __builtin_bit_cast(float, u & 0xffff0000u); }
__device__ __forceinline__ unsigned pk(float lo, float hi) {
  f32x2 v = {lo, hi};
  return __builtin_bit_cast(unsigned, __builtin_convertvector(v, bf16x2));
}
__device__ __forceinline__ float rbf(float f) { return bflo(pk(f, f)); }
template <int CTRL> __device__ __forceinline__ float dpp(float x) {
  return __builtin_bit_cast(float, __builtin_amdgcn_mov_dpp(__builtin_bit_cast(int, x), CTRL, 0xf, 0xf, true));
}
constexpr int ROR1 = 0x121, ROR2 = 0x122, ROR4 = 0x124, ROR8 = 0x128;
__device__ __forceinline__ float x16_sum(float x) {
  auto s = __builtin_amdgcn_permlane16_swap(__float_as_uint(x), __float_as_uint(x), false, false);
  return __uint_as_float(s[0]) + __uint_as_float(s[1]);
}
__device__ __forceinline__ float x32_sum(float x) {
  auto s = __builtin_amdgcn_permlane32_swap(__float_as_uint(x), __float_as_uint(x), false, false);
  return __uint_as_float(s[0]) + __uint_as_float(s[1]);
}
__device__ __forceinline__ float x16_max(float x) {
  auto s = __builtin_amdgcn_permlane16_swap(__float_as_uint(x), __float_as_uint(x), false, false);
  return fmaxf(__uint_as_float(s[0]), __uint_as_float(s[1]));
}
__device__ __forceinline__ float x32_max(float x) {
  auto s = __builtin_amdgcn_permlane32_swap(__float_as_uint(x), __float_as_uint(x), false, false);
  return fmaxf(__uint_as_float(s[0]), __uint_as_float(s[1]));
}
__device__ __forceinline__ float wave_sum(float x) {
  x += dpp<ROR8>(x); x += dpp<ROR4>(x); x += dpp<ROR2>(x); x += dpp<ROR1>(x);
  return x32_sum(x16_sum(x));
}
__device__ __forceinline__ float wave_max(float x) {
  x = fmaxf(x, dpp<ROR8>(x)); x = fmaxf(x, dpp<ROR4>(x)); x = fmaxf(x, dpp<ROR2>(x)); x = fmaxf(x, dpp<ROR1>(x));
  return x32_max(x16_max(x));
}
__device__ __forceinline__ f32x4 mfma32(v4u a, v4u b, f32x4 c) {
  return __builtin_amdgcn_mfma_f32_16x16x32_bf16(__builtin_bit_cast(bf16x8, a), __builtin_bit_cast(bf16x8, b), c, 0, 0, 0);
}
__device__ __forceinline__ f32x4 mfma16(v2u a, v2u b, f32x4 c) {
  return __builtin_amdgcn_mfma_f32_16x16x16bf16_1k(__builtin_bit_cast(s16x4, a), __builtin_bit_cast(s16x4, b), c, 0, 0, 0);
}
__device__ __forceinline__ v2u trd(LAS unsigned char* a) {
  return __builtin_bit_cast(v2u, __builtin_amdgcn_ds_read_tr16_b64_v4i16((LAS s16x4*)a));
}
__device__ __forceinline__ v4u trd2(LAS unsigned char* a0, LAS unsigned char* a1) {
  const v2u lo = trd(a0), hi = trd(a1);
  return v4u{lo.x, lo.y, hi.x, hi.y};
}
__device__ __forceinline__ v4u layernorm_row(v4u v, bool act) {
  float f[8];
#pragma unroll
  for (int i = 0; i < 4; ++i) { f[2 * i] = bflo(v[i]); f[2 * i + 1] = bfhi(v[i]); }
  const float x0 = __builtin_bit_cast(float, __builtin_amdgcn_readfirstlane(__builtin_bit_cast(int, f[0])));
  float s1 = 0.f, s2 = 0.f;
#pragma unroll
  for (int i = 0; i < 8; ++i) { const float d = f[i] - x0; s1 += d; s2 = fmaf(d, d, s2); }
  s1 = act ? s1 : 0.f; s2 = act ? s2 : 0.f;
  s1 = wave_sum(s1); s2 = wave_sum(s2);
  const float md = s1 * (1.0f / D);
  const float var = fmaxf(fmaf(-md, md, s2 * (1.0f / D)), 0.f);
  const float m = rbf(x0 + md);
  const float den = rbf(__builtin_amdgcn_sqrtf(rbf(rbf(var) + 1e-5f)));
  const float inv = __builtin_amdgcn_rcpf(den);
  v4u o;
#pragma unroll
  for (int i = 0; i < 4; ++i) {
    const unsigned d = pk(f[2 * i] - m, f[2 * i + 1] - m);
    o[i] = pk(bflo(d) * inv, bfhi(d) * inv);
  }
  return o;
}
__device__ __forceinline__ f32x4 sel4(int sub, f32x4 a0, f32x4 a1, f32x4 a2, f32x4 a3) {
  const f32x4 lo = (sub & 1) ? a1 : a0, hi = (sub & 1) ? a3 : a2;
  return (sub & 2) ? hi : lo;
}
__device__ __forceinline__ v4u gran2(float a, float b, unsigned tag) { return v4u{__float_as_uint(a), tag, __float_as_uint(b), tag}; }

__global__ void __launch_bounds__(512, 2) fused_decode_block(Params p) {
  __shared__ __attribute__((aligned(16))) unsigned char smem[L_END];
  LAS unsigned char* L = (LAS unsigned char*)smem;
  const int c = blockIdx.x, tid = threadIdx.x, lane = tid & 63, w = __builtin_amdgcn_readfirstlane(tid >> 6);
  const int kvh = c & 7, j = c >> 3, b = j >> 3, s = j & 7;
  const int n = lane & 15, g = lane >> 4;
  const __amdgpu_buffer_rsrc_t rs = __builtin_amdgcn_make_buffer_rsrc((void*)p.ws, 0, WS_END, 0x00020000);
  STAMP_DECL;
  STAMP(0);

  const unsigned ep = __builtin_amdgcn_raw_buffer_load_b32(rs, WS_EPOCH, 0, SC1);
  const v4u xr = *(const GAS v4u*)(p.x + (w & 3) * D + (lane < 56 ? lane : 55) * 8);
  const int w7 = w < 7 ? w : 6;
  const char* wqp = (const char*)p.wq + (size_t)(b * 112 + w7 * 16 + (lane >> 3)) * 8192 + (kvh * 512 + s * 64) * 2 + (lane & 7) * 16;
  const v4u wq0 = ldnt(wqp), wq1 = ldnt(wqp + 8 * 8192);
  SB();
  const size_t kvoff = ((size_t)(b * 8 + kvh) * SK + (SK - WIN)) * 512 + (size_t)(s + 8 * w) * 1024 + lane * 16;
  const char* kp = (const char*)p.kc + kvoff;
  const char* vp = (const char*)p.vc + kvoff;
  v4u K[8], V[8];
#pragma unroll
  for (int i = 0; i < 4; ++i) K[i] = ldnt(kp + i * 65536);
  SB();
  const unsigned tag = __builtin_amdgcn_readfirstlane(ep) + 1u;

  if (w >= 4) {
    const v4u xn = layernorm_row(xr, lane < 56);
    if (lane < 56) *(LAS v4u*)(L + L_XN + (w & 3) * XN_STR + lane * 16) = xn;
  }
  if (w < 7) {
    const int r0 = w * 16 + (lane >> 3), ch = lane & 7;
    *(LAS v4u*)(L + L_WQ + r0 * 128 + ((ch ^ (((r0 >> 1) & 3) << 1)) << 4)) = wq0;
    *(LAS v4u*)(L + L_WQ + (r0 + 8) * 128 + ((ch ^ (((r0 >> 1) & 3) << 1)) << 4)) = wq1;
  }
  STAMP(1);
  WG_BARRIER();
  if (w < 2) {
    f32x4 a0 = {0.f, 0.f, 0.f, 0.f}, a1 = a0;
    const int row = 4 * g + (n >> 2), sw = (row >> 1) & 3;
    LAS unsigned char* t0 = L + L_WQ + row * 128 + (((2 * w) ^ sw) << 5) + (n & 3) * 8;
    LAS unsigned char* t1 = L + L_WQ + row * 128 + (((2 * w + 1) ^ sw) << 5) + (n & 3) * 8;
    LAS unsigned char* xb = L + L_XN + (n >> 2) * XN_STR + (b * 112 + 4 * g) * 2;
#pragma unroll
    for (int ks = 0; ks < 7; ++ks) {
      const v2u xf = *(LAS v2u*)(xb + ks * 32);
      a0 = mfma16(trd(t0 + ks * 2048), xf, a0);
      a1 = mfma16(trd(t1 + ks * 2048), xf, a1);
    }
    const f32x4 a = (n & 2) ? a1 : a0;
    const float e0 = (n & 1) ? a[2] : a[0], e1 = (n & 1) ? a[3] : a[1];
    const int off = WS_GQ + (((kvh * 4 + (n >> 2)) * 4 + b) * 256 + (s * 2 + w) * 16 + g * 4 + (n & 3)) * 16;
    __builtin_amdgcn_raw_buffer_store_b128(gran2(e0, e1, tag), rs, off, 0, SC1);
  }
  STAMP(2);

  LAS unsigned char* kw = L + L_K + w * 8192;
  const int kch = lane & 31, khalf = lane >> 5;
#pragma unroll
  for (int i = 0; i < 4; ++i) {
    const int key = 2 * i + khalf;
    *(LAS v4u*)(kw + key * 512 + (((kch & 16) | ((kch ^ key) & 15)) << 4)) = K[i];
    SB();
    K[4 + i] = ldnt(kp + (4 + i) * 65536);
    SB();
  }
  const int gqoff = WS_GQ + ((kvh * 4 + b) * 4) * 4096 + tid * 8;
  v2u qg[4];
#pragma unroll
  for (int i = 4; i < 8; ++i) {
    const int key = 2 * i + khalf;
    *(LAS v4u*)(kw + key * 512 + (((kch & 16) | ((kch ^ key) & 15)) << 4)) = K[i];
    SB();
    V[i - 4] = ldnt(vp + (i - 4) * 65536);
    SB();
    if (i == 4) {
#pragma unroll
      for (int k = 0; k < 4; ++k) qg[k] = __builtin_amdgcn_raw_buffer_load_b64(rs, gqoff + k * 4096, 0, SC1);
      SB();
    }
  }
  STAMP(3);
  float qv = (__uint_as_float(qg[0].x) + __uint_as_float(qg[1].x)) + (__uint_as_float(qg[2].x) + __uint_as_float(qg[3].x));
  if (__builtin_expect(!__all((qg[0].y == tag) & (qg[1].y == tag) & (qg[2].y == tag) & (qg[3].y == tag)), 0)) {
    for (unsigned spins = 0;; ) {
      MEMFENCE();
#pragma unroll
      for (int k = 0; k < 4; ++k) qg[k] = __builtin_amdgcn_raw_buffer_load_b64(rs, gqoff + k * 4096, 0, SC1);
      qv = (__uint_as_float(qg[0].x) + __uint_as_float(qg[1].x)) + (__uint_as_float(qg[2].x) + __uint_as_float(qg[3].x));
      const bool ok = (qg[0].y == tag) & (qg[1].y == tag) & (qg[2].y == tag) & (qg[3].y == tag);
      if (__all(ok) || ++spins > SPIN_CAP) break;
    }
  }
  {
    const int pos = tid >> 1, sub = pos & 3, qq = (pos >> 2) & 3, tp = pos >> 4;
    const int col = 32 * tp + 16 * (sub >> 1) + 4 * qq + 2 * (sub & 1) + (tid & 1);
    *(LAS u16*)(L + L_Q + (col >> 8) * Q_STR + (col & 255) * 2) = (u16)pk(qv, qv);
  }
  SB();
  V[4] = ldnt(vp + 4 * 65536);
  SB();
  STAMP(4);
  WG_BARRIER();
  {
    f32x4 a = {0.f, 0.f, 0.f, 0.f};
#pragma unroll
    for (int t = 0; t < 8; ++t) {
      const int ch = 4 * t + g;
      const v4u kf = *(LAS v4u*)(kw + n * 512 + (((ch & 16) | ((ch ^ n) & 15)) << 4));
      const v4u qf = *(LAS v4u*)(L + L_Q + (n & 1) * Q_STR + ch * 16);
      a = mfma32(qf, kf, a);
    }
    if (lane < 16) *(LAS unsigned*)(L + L_SC + (16 * (n >> 1) + 2 * w + (n & 1)) * 4) = pk(a[0], a[1]);
  }
  WG_BARRIER();
  const int vch = lane & 31;
#define STAGE_V(i) do { const int row_ = 16 * (i) + 2 * w + khalf; \
    *(LAS v4u*)(L + L_V + row_ * 512 + ((vch ^ ((((row_ & 3) << 1) | ((row_ >> 3) & 1)) << 1)) << 4)) = V[i]; } while (0)
  STAGE_V(0); STAGE_V(1);
  SB();
  V[5] = ldnt(vp + 5 * 65536);
  SB();
  LAS unsigned char* pw = L + L_P + w * 640;
  {
    const v2u sc = *(LAS v2u*)(L + L_SC + lane * 8);
    const float s00 = bflo(sc.x) * 0.0625f, s10 = bfhi(sc.x) * 0.0625f, s01 = bflo(sc.y) * 0.0625f, s11 = bfhi(sc.y) * 0.0625f;
    const float m0 = wave_max(fmaxf(s00, s01)), m1 = wave_max(fmaxf(s10, s11));
    const float e00 = __builtin_amdgcn_exp2f((s00 - m0) * LOG2E), e01 = __builtin_amdgcn_exp2f((s01 - m0) * LOG2E);
    const float e10 = __builtin_amdgcn_exp2f((s10 - m1) * LOG2E), e11 = __builtin_amdgcn_exp2f((s11 - m1) * LOG2E);
    *(LAS unsigned*)(pw + lane * 4) = pk(e00, e01);
    *(LAS unsigned*)(pw + P_STR + lane * 4) = pk(e10, e11);
    if (w == 7) {
      const float l0 = wave_sum(e00 + e01), l1 = wave_sum(e10 + e11);
      if (lane < 2) __builtin_amdgcn_raw_buffer_store_b128(gran2(lane ? m1 : m0, lane ? l1 : l0, tag), rs, WS_GS + (kvh * 32 + j) * 32 + lane * 16, 0, SC1);
    }
  }
  STAMP(5);
  STAGE_V(2);
  SB();
  V[6] = ldnt(vp + 6 * 65536);
  SB();
  STAGE_V(3);
  SB();
  V[7] = ldnt(vp + 7 * 65536);
  SB();
  STAGE_V(4); STAGE_V(5);
  v4u pf[4];
#pragma unroll
  for (int t = 0; t < 4; ++t) pf[t] = *(LAS v4u*)(pw + (n & 1) * P_STR + t * 64 + g * 16);
  WG_BARRIER();
  f32x4 o0 = {0.f, 0.f, 0.f, 0.f}, o1 = o0;
  const int vq = n >> 2, vsw = (vq << 1) | (g & 1);
  LAS unsigned char* v0 = L + L_V + (8 * g + vq) * 512 + (((2 * w) ^ vsw) << 5) + (n & 3) * 8;
  LAS unsigned char* v1 = L + L_V + (8 * g + vq) * 512 + (((2 * w + 1) ^ vsw) << 5) + (n & 3) * 8;
#pragma unroll
  for (int t = 0; t < 3; ++t) {
    o0 = mfma32(trd2(v0 + t * 16384, v0 + t * 16384 + 2048), pf[t], o0);
    o1 = mfma32(trd2(v1 + t * 16384, v1 + t * 16384 + 2048), pf[t], o1);
  }
  SB();
  STAGE_V(6); STAGE_V(7);
  STAMP(6);
  WG_BARRIER();
  o0 = mfma32(trd2(v0 + 3 * 16384, v0 + 3 * 16384 + 2048), pf[3], o0);
  o1 = mfma32(trd2(v1 + 3 * 16384, v1 + 3 * 16384 + 2048), pf[3], o1);
  {
    const f32x4 a = (n & 2) ? o1 : o0;
    const float e0 = (n & 4) ? a[2] : a[0], e1 = (n & 4) ? a[3] : a[1];
    if (n < 8) __builtin_amdgcn_raw_buffer_store_b128(gran2(e0, e1, tag), rs, WS_GC + ((kvh * 8 + w) * 32 + j) * 512 + (g * 8 + n) * 16, 0, SC1);
  }
  STAMP(7);

  const int lrow = lane >> 3, lch = lane & 7;
  const int tsw = ((vq >> 1) & 1) | ((g & 1) << 1);
#define LT_WADDR(base, row) ((base) + (row) * 128 + ((lch ^ (((((row) >> 1) & 1) | ((((row) >> 3) & 1) << 1)) << 1)) << 4))
#define LT_RADDR(base, ks, t, h) ((base) + (32 * (ks) + 8 * g + 4 * (h) + vq) * 128 + (((t) ^ tsw) << 5) + (n & 3) * 8)

  if (j >= 8) {
    const int rsl = j & 7, cg = (j >> 3) - 1;
    const int nline = cg == 0 ? 3 : 2, line0 = cg == 0 ? 0 : 2 * cg + 1;
    v4u wf[2][4];
    if (w >= 4 && w - 4 < nline) {
      const char* wop = (const char*)p.wo + (size_t)(kvh * 512 + rsl * 32 + lrow) * 896 + (line0 + w - 4) * 128 + lch * 16;
      v4u t[8];
#pragma unroll
      for (int i = 0; i < 8; ++i) t[i] = ldnt(wop + ((i & 3) * 8 + (i >> 2) * 256) * 896);
      SB();
#pragma unroll
      for (int i = 0; i < 8; ++i) *(LAS v4u*)LT_WADDR(kw, 8 * i + lrow) = t[i];
      LDS_WAIT();
#pragma unroll
      for (int ks = 0; ks < 2; ++ks)
#pragma unroll
        for (int t4 = 0; t4 < 4; ++t4) wf[ks][t4] = trd2(LT_RADDR(kw, ks, t4, 0), LT_RADDR(kw, ks, t4, 1));
    } else {
#pragma unroll
      for (int ks = 0; ks < 2; ++ks)
#pragma unroll
        for (int t4 = 0; t4 < 4; ++t4) wf[ks][t4] = v4u{0u, 0u, 0u, 0u};
    }
    if (w < 4) {
      v4u st;
      for (unsigned spins = 0;; ) {
        MEMFENCE();
        st = __builtin_amdgcn_raw_buffer_load_b128(rs, WS_GS + kvh * 1024 + lane * 16, 0, SC1);
        if (__all((st.y == tag) & (st.w == tag)) || ++spins > SPIN_CAP) break;
      }
      const float m = __uint_as_float(st.x), l = __uint_as_float(st.z);
      float M = fmaxf(m, dpp<ROR2>(m)); M = fmaxf(M, dpp<ROR4>(M)); M = fmaxf(M, dpp<ROR8>(M));
      const float e = __builtin_amdgcn_exp2f((m - M) * LOG2E);
      float Ls = l * e; Ls += dpp<ROR2>(Ls); Ls += dpp<ROR4>(Ls); Ls += dpp<ROR8>(Ls);
      const float wt = e * __builtin_amdgcn_rcpf(Ls);
      float cw[4];
#pragma unroll
      for (int u = 0; u < 4; ++u) cw[u] = __shfl(wt, 16 * w + 4 * u + 2 * khalf + (lane & 1));
      v4u v[4];
      const int off = WS_GC + ((kvh * 8 + rsl) * 32 + 8 * w) * 512 + lane * 16;
      for (unsigned spins = 0;; ) {
        MEMFENCE();
        bool ok = true;
#pragma unroll
        for (int u = 0; u < 4; ++u) v[u] = __builtin_amdgcn_raw_buffer_load_b128(rs, off + u * 1024, 0, SC1);
#pragma unroll
        for (int u = 0; u < 4; ++u) ok &= (v[u].y == tag) & (v[u].w == tag);
        if (__all(ok) || ++spins > SPIN_CAP) break;
      }
      float a0 = 0.f, a1 = 0.f;
#pragma unroll
      for (int u = 0; u < 4; ++u) { a0 = fmaf(cw[u], __uint_as_float(v[u].x), a0); a1 = fmaf(cw[u], __uint_as_float(v[u].z), a1); }
      a0 = x32_sum(a0); a1 = x32_sum(a1);
      const int pn = lane & 7, pq = (lane >> 3) & 3;
      const int kap = (pn & 1) * 32 + 16 * ((pn >> 1) & 1) + 4 * pq + 2 * (pn >> 2);
      if (lane < 32) *(LAS unsigned*)(L + L_OB + w * OB_STR + kap * 2) = pk(a0, a1);
    }
    STAMP(8);
    WG_BARRIER();
    if (w >= 4 && w - 4 < nline) {
      const v4u b0 = *(LAS v4u*)(L + L_OB + vq * OB_STR + g * 16), b1 = *(LAS v4u*)(L + L_OB + vq * OB_STR + 64 + g * 16);
      f32x4 acc[4];
#pragma unroll
      for (int t4 = 0; t4 < 4; ++t4) acc[t4] = mfma32(wf[0][t4], b0, f32x4{0.f, 0.f, 0.f, 0.f});
#pragma unroll
      for (int t4 = 0; t4 < 4; ++t4) acc[t4] = mfma32(wf[1][t4], b1, acc[t4]);
      const f32x4 a = sel4(n & 3, acc[0], acc[1], acc[2], acc[3]);
      const int off = WS_G1 + (((vq * 7 + line0 + w - 4) * 4 + g) * 64 + kvh * 8 + rsl) * 128 + (n & 3) * 16;
      __builtin_amdgcn_raw_buffer_store_b128(gran2(a[0], a[1], tag), rs, off, 0, SC1);
      __builtin_amdgcn_raw_buffer_store_b128(gran2(a[2], a[3], tag), rs, off + 64, 0, SC1);
    }
    STAMP(9);
    if (w == 7 && j >= 16 && j < 30) {
      const int r = (j - 16) * 8 + kvh;
      const int rq = r & 3, rl = (r >> 2) % 7, rb = r / 28;
      const int col = 64 * rl + 16 * (lane & 3) + 4 * rq + 2 * ((lane >> 2) & 1);
      const unsigned xin = *(const GAS unsigned*)(p.x + rb * D + col);
      float a0, a1;
      for (unsigned spins = 0;; ) {
        MEMFENCE();
        v4u v[8]; bool ok = true;
#pragma unroll
        for (int i = 0; i < 8; ++i) v[i] = __builtin_amdgcn_raw_buffer_load_b128(rs, WS_G1 + r * 8192 + i * 1024 + lane * 16, 0, SC1);
        a0 = 0.f; a1 = 0.f;
#pragma unroll
        for (int i = 0; i < 8; ++i) { ok &= (v[i].y == tag) & (v[i].w == tag); a0 += __uint_as_float(v[i].x); a1 += __uint_as_float(v[i].z); }
        if (__all(ok) || ++spins > SPIN_CAP) break;
      }
      a0 += dpp<ROR8>(a0); a1 += dpp<ROR8>(a1);
      a0 = x32_sum(x16_sum(a0)); a1 = x32_sum(x16_sum(a1));
      const unsigned at = pk(a0, a1);
      const unsigned h = pk(bflo(xin) + bflo(at), bfhi(xin) + bfhi(at));
      if (lane < 8) __builtin_amdgcn_raw_buffer_store_b64(v2u{h, tag}, rs, WS_G2 + r * 64 + lane * 8, 0, SC1);
      if (r == 0 && lane == 0) __builtin_amdgcn_raw_buffer_store_b32(tag, rs, WS_EPOCH, 0, SC1);
      STAMP(10);
      for (unsigned spins = 0;; ) {
        MEMFENCE();
        v4u v[7]; bool ok = true;
#pragma unroll
        for (int i = 0; i < 7; ++i) v[i] = __builtin_amdgcn_raw_buffer_load_b128(rs, WS_G3 + r * 7168 + i * 1024 + lane * 16, 0, SC1);
        a0 = 0.f; a1 = 0.f;
#pragma unroll
        for (int i = 0; i < 7; ++i) { ok &= (v[i].y == tag) & (v[i].w == tag); a0 += __uint_as_float(v[i].x); a1 += __uint_as_float(v[i].z); }
        if (__all(ok) || ++spins > SPIN_CAP) break;
      }
      a0 += dpp<ROR8>(a0); a1 += dpp<ROR8>(a1);
      a0 = x32_sum(x16_sum(a0)); a1 = x32_sum(x16_sum(a1));
      const unsigned ff = pk(a0, a1);
      if (lane < 8) __hip_atomic_store((GAS unsigned*)(p.out + rb * D + col), pk(bflo(h) + bflo(ff), bfhi(h) + bfhi(ff)), __ATOMIC_RELAXED, __HIP_MEMORY_SCOPE_AGENT);
      STAMP(11);
    }
  } else if (j < 7) {
    const int f = kvh * 7 + j;
    v4u df[4];
    WG_BARRIER();
    if (w < 7) {
      const size_t go = (size_t)(w * 64 + (lane >> 2)) * (FF * 2) + f * 64 + (lane & 3) * 16;
      const char* wdp = (const char*)p.wd + (size_t)(f * 32 + lrow) * 896 + w * 128 + lch * 16;
      v4u tg[4], tu[4], td[4];
#pragma unroll
      for (int i = 0; i < 4; ++i) { tg[i] = ldnt((const char*)p.wg + go + (size_t)i * 16 * FF * 2); tu[i] = ldnt((const char*)p.wu + go + (size_t)i * 16 * FF * 2); }
#pragma unroll
      for (int i = 0; i < 4; ++i) td[i] = ldnt(wdp + i * 8 * 896);
      SB();
#pragma unroll
      for (int i = 0; i < 4; ++i) {
        const int row = w * 64 + i * 16 + (lane >> 2), m = ((row >> 1) & 1) | (((row >> 3) & 1) << 1);
        *(LAS v4u*)(L + L_K + row * 128 + (((lane & 3) ^ m) << 4)) = tg[i];
        *(LAS v4u*)(L + L_K + row * 128 + 64 + (((lane & 3) ^ m) << 4)) = tu[i];
      }
      LAS unsigned char* dw = L + L_V + w * 4096;
#pragma unroll
      for (int i = 0; i < 4; ++i) *(LAS v4u*)LT_WADDR(dw, 8 * i + lrow) = td[i];
      LDS_WAIT();
#pragma unroll
      for (int t4 = 0; t4 < 4; ++t4) df[t4] = trd2(LT_RADDR(dw, 0, t4, 0), LT_RADDR(dw, 0, t4, 1));
    } else {
#pragma unroll
      for (int t4 = 0; t4 < 4; ++t4) df[t4] = v4u{0u, 0u, 0u, 0u};
    }
    WG_BARRIER();
    STAMP(8);
    v4u gf[14];
    if (w < 4) {
      LAS unsigned char* gb = L + L_K + (8 * g + vq) * 128 + ((n & 2) << 5) + ((w ^ tsw) << 4) + (n & 1) * 8;
#pragma unroll
      for (int ks = 0; ks < 14; ++ks) gf[ks] = trd2(gb + ks * 4096, gb + ks * 4096 + 512);
    } else {
#pragma unroll
      for (int ks = 0; ks < 14; ++ks) gf[ks] = v4u{0u, 0u, 0u, 0u};
      const int l56 = lane < 56 ? lane : 55;
      v4u h0, h1;
      for (unsigned spins = 0;; ) {
        MEMFENCE();
        h0 = __builtin_amdgcn_raw_buffer_load_b128(rs, WS_G2 + (w - 4) * 1792 + l56 * 16, 0, SC1);
        h1 = __builtin_amdgcn_raw_buffer_load_b128(rs, WS_G2 + (w - 4) * 1792 + 896 + l56 * 16, 0, SC1);
        if (__all((h0.y == tag) & (h0.w == tag) & (h1.y == tag) & (h1.w == tag)) || ++spins > SPIN_CAP) break;
      }
      STAMP(9);
      const v4u hn = layernorm_row(v4u{h0.x, h0.z, h1.x, h1.z}, lane < 56);
      if (lane < 56) {
        LAS unsigned char* hb = L + L_XN + (w - 4) * XN_STR;
        const int p0 = lane, p1 = lane + 56;
        const int c0 = 64 * (p0 >> 4) + 4 * ((p0 >> 2) & 3) + 2 * ((p0 >> 1) & 1) + 32 * (p0 & 1);
        const int c1 = 64 * (p1 >> 4) + 4 * ((p1 >> 2) & 3) + 2 * ((p1 >> 1) & 1) + 32 * (p1 & 1);
        *(LAS unsigned*)(hb + c0 * 2) = hn.x; *(LAS unsigned*)(hb + c0 * 2 + 32) = hn.y;
        *(LAS unsigned*)(hb + c1 * 2) = hn.z; *(LAS unsigned*)(hb + c1 * 2 + 32) = hn.w;
      }
    }
    WG_BARRIER();
    if (w < 4) {
      f32x4 a = {0.f, 0.f, 0.f, 0.f};
      LAS unsigned char* hb = L + L_XN + vq * XN_STR + g * 16;
#pragma unroll
      for (int ks = 0; ks < 14; ++ks) a = mfma32(gf[ks], *(LAS v4u*)(hb + ks * 64), a);
      const float lo = (n & 1) ? a[1] : a[0], hi = (n & 1) ? a[3] : a[2];
      const float mine = rbf((n & 2) ? hi : lo);
      auto sw = __builtin_amdgcn_permlane32_swap(__float_as_uint(mine), __float_as_uint(mine), false, false);
      const float gt = __uint_as_float(sw[0]), up = __uint_as_float(sw[1]);
      const float sg = rbf(gt * __builtin_amdgcn_rcpf(1.0f + __builtin_amdgcn_exp2f(-gt * LOG2E)));
      const float av = sg * up;
      if (lane < 32) *(LAS u16*)(L + L_ACT + vq * 64 + (8 * w + 4 * g + (n & 3)) * 2) = (u16)pk(av, av);
    }
    WG_BARRIER();
    if (w < 7) {
      const v4u af = *(LAS v4u*)(L + L_ACT + vq * 64 + g * 16);
      f32x4 acc[4];
#pragma unroll
      for (int t4 = 0; t4 < 4; ++t4) acc[t4] = mfma32(df[t4], af, f32x4{0.f, 0.f, 0.f, 0.f});
      const f32x4 a = sel4(n & 3, acc[0], acc[1], acc[2], acc[3]);
      const int off = WS_G3 + (((vq * 7 + w) * 4 + g) * 56 + f) * 128 + (n & 3) * 16;
      __builtin_amdgcn_raw_buffer_store_b128(gran2(a[0], a[1], tag), rs, off, 0, SC1);
      __builtin_amdgcn_raw_buffer_store_b128(gran2(a[2], a[3], tag), rs, off + 64, 0, SC1);
    }
    STAMP(10);
  }
  STAMP_FLUSH();
}

extern "C" void kernel_launch(void* const* d_in, const int* in_sizes, int n_in, void* d_out, int out_size, void* d_ws, size_t ws_size,
                              hipStream_t stream) {
  Params p;
  p.x = (const u16*)d_in[0]; p.kc = (const u16*)d_in[1]; p.vc = (const u16*)d_in[2];
  p.wq = (const u16*)d_in[3]; p.wo = (const u16*)d_in[4];
  p.wg = (const u16*)d_in[6]; p.wu = (const u16*)d_in[7]; p.wd = (const u16*)d_in[8];
  p.out = (u16*)d_out; p.ws = (unsigned char*)d_ws;
  fused_decode_block<<<256, 512, 0, stream>>>(p);
}
```

```cpp
#include <hip/hip_runtime.h>
#include <hip/hip_bf16.h>

typedef unsigned short u16;
typedef unsigned long long u64;
typedef unsigned v4u __attribute__((ext_vector_type(4)));
typedef unsigned v2u __attribute__((ext_vector_type(2)));
typedef float f32x4 __attribute__((ext_vector_type(4)));
typedef float f32x2 __attribute__((ext_vector_type(2)));
typedef short s16x4 __attribute__((ext_vector_type(4)));
typedef __bf16 bf16x8 __attribute__((ext_vector_type(8)));
typedef __bf16 bf16x2 __attribute__((ext_vector_type(2)));
#define GAS __attribute__((address_space(1)))
#define LAS __attribute__((address_space(3)))

#define QG_STAGE 3
#define T_HC 180
#define D_H1 82
#define D_H2 50
#define D_H3 108
#define D_GU 15
#define D_ST 35
#define D_PF 70
#define SHADOW 1
#define D_SH 40
#define D_WD 50
#define TLB_WARM 1
#define OUT_TOUCH 1
#define OUT_SC1 0
#define WO_POS 0
#define PF_POS 0
#define PF_SLEEP 3
#define PF_OWN 4
#define PF_MODE 1
#define PF_IN 2
#define DET_WG 63
#define PW3 2
#define D_LATE 40
#define T_GU (T_HC + D_GU)
#define T_H1 (T_HC + D_H1)
#define T_H2 (T_H1 + D_H2)
#define T_H3 (T_H2 + D_H3)
#define PACE 63
#define PACE0 50
#define PACE1 30
#define D_QG 95
#define PREWRITE 1
#define K_DELAY 3
#define Q_DELAY 0
#define LWIN_F 8
#define CODE_BYTES 13600
#define SL_C 0
#define SL_R 0
#define SL_H 0
#define LWIN 8

constexpr int D = 448, SK = 4096, WIN = 1024, FF = 1792;
#define POL 0
constexpr size_t WS_ALIGN = (2u << 20) - 1;
constexpr int WS_EPOCH = 0;
constexpr int WS_ROT = 1024 + 8 * 128;
constexpr int WS_T0 = 1024;
constexpr int WS_PRB = 16384;
constexpr int WS_G2 = 32768 + POL * 8192;
constexpr int WS_GS = WS_G2 + 16384;
constexpr int WS_GC = WS_GS + 16384;
constexpr int WS_G1 = WS_GC + 8 * 8 * 32 * 256;
constexpr int WS_G3 = WS_G1 + 128 * 64 * 64;
constexpr int WS_GQ = WS_G3 + 128 * 64 * 64;
constexpr int WS_END = WS_GQ + 8 * 4 * 4 * 512 * 8;
static_assert(WS_GQ <= (2 << 20), "one 2 MB page");
constexpr int WS_STAMP = 8 << 20;
constexpr int L_K = 0, L_V = 65536, L_WQ = 131072, L_XN = L_WQ + 14336, XN_STR = 960;
constexpr int L_Q = L_XN + 3840, Q_STR = 576;
constexpr int L_P = L_Q + 1152, P_STR = 320;
constexpr int L_OB = L_P + 640, OB_STR = 192;
constexpr int L_ACT = L_OB + 768;
constexpr int L_ST = L_ACT + 256;
constexpr int L_SC = L_ST + 32;
constexpr int L_FL = L_SC + 512;
constexpr int L_EP = L_FL + 64;
constexpr int L_END = L_EP + 1024 + 256;
constexpr int L_GU = L_K, L_WD = L_V;
static_assert(L_END <= 163840, "LDS");
constexpr unsigned SPIN_CAP = 1u << 16;
constexpr int SC1 = 16;
constexpr float LOG2E = 1.4426950408889634f;

struct Params {
  const u16 *wq, *x;
  unsigned char* ws;
  const u16 *kc, *vc, *wg, *wu;
  const u16 *wo, *wd;
  u16* out;
};

#define SB() __builtin_amdgcn_sched_barrier(0)
#define MEMFENCE() asm volatile("" ::: "memory")
#define WG_BARRIER() asm volatile("s_waitcnt lgkmcnt(0)\n\ts_barrier" ::: "memory")
#define PIN(x) asm volatile("" : "+v"(x))
#define VM_WAIT(N) asm volatile("s_waitcnt vmcnt(" #N ")" ::: "memory")

#define SAFE_NOP ""
#define STAMP(i) do {} while (0)
#define COUNT(i, v) do {} while (0)
#define STAMP_DECL do {} while (0)
#define STAMP_FLUSH() do {} while (0)

__device__ __forceinline__ float bflo(unsigned u) { return __builtin_bit_cast(float, u << 16); }
__device__ __forceinline__ float bfhi(unsigned u) { return __builtin_bit_cast(float, u & 0xffff0000u); }
__device__ __forceinline__ unsigned pk(float lo, float hi) {
  f32x2 v = {lo, hi};
  return __builtin_bit_cast(unsigned, __builtin_convertvector(v, bf16x2));
}
__device__ __forceinline__ float dot2(unsigned a, unsigned b, float c) {
  return __builtin_amdgcn_fdot2_f32_bf16(__builtin_bit_cast(bf16x2, a), __builtin_bit_cast(bf16x2, b), c, false);
}
__device__ __forceinline__ float rbf(float f) { return bflo(pk(f, f)); }
template <int CTRL> __device__ __forceinline__ float dpp(float x) {
  return __builtin_bit_cast(float, __builtin_amdgcn_mov_dpp(__builtin_bit_cast(int, x), CTRL, 0xf, 0xf, true));
}
constexpr int ROR1 = 0x121, ROR2 = 0x122, ROR4 = 0x124, ROR8 = 0x128;
__device__ __forceinline__ float x16_sum(float x) {
  auto s = __builtin_amdgcn_permlane16_swap(__float_as_uint(x), __float_as_uint(x), false, false);
  return __uint_as_float(s[0]) + __uint_as_float(s[1]);
}
__device__ __forceinline__ float x32_sum(float x) {
  auto s = __builtin_amdgcn_permlane32_swap(__float_as_uint(x), __float_as_uint(x), false, false);
  return __uint_as_float(s[0]) + __uint_as_float(s[1]);
}
__device__ __forceinline__ float x16_max(float x) {
  auto s = __builtin_amdgcn_permlane16_swap(__float_as_uint(x), __float_as_uint(x), false, false);
  return fmaxf(__uint_as_float(s[0]), __uint_as_float(s[1]));
}
__device__ __forceinline__ float x32_max(float x) {
  auto s = __builtin_amdgcn_permlane32_swap(__float_as_uint(x), __float_as_uint(x), false, false);
  return fmaxf(__uint_as_float(s[0]), __uint_as_float(s[1]));
}
__device__ __forceinline__ float wave_sum(float x) {
  x += dpp<ROR8>(x); x += dpp<ROR4>(x); x += dpp<ROR2>(x); x += dpp<ROR1>(x);
  return x32_sum(x16_sum(x));
}
__device__ __forceinline__ float wave_max(float x) {
  x = fmaxf(x, dpp<ROR8>(x)); x = fmaxf(x, dpp<ROR4>(x)); x = fmaxf(x, dpp<ROR2>(x)); x = fmaxf(x, dpp<ROR1>(x));
  return x32_max(x16_max(x));
}
__device__ __forceinline__ f32x4 mfma32(v4u a, v4u b, f32x4 c) {
  return __builtin_amdgcn_mfma_f32_16x16x32_bf16(__builtin_bit_cast(bf16x8, a), __builtin_bit_cast(bf16x8, b), c, 0, 0, 0);
}
__device__ __forceinline__ f32x4 mfma16(v2u a, v2u b, f32x4 c) {
  return __builtin_amdgcn_mfma_f32_16x16x16bf16_1k(__builtin_bit_cast(s16x4, a), __builtin_bit_cast(s16x4, b), c, 0, 0, 0);
}
__device__ __forceinline__ v2u trd(LAS unsigned char* a) {
  return __builtin_bit_cast(v2u, __builtin_amdgcn_ds_read_tr16_b64_v4i16((LAS s16x4*)a));
}
__device__ __forceinline__ v4u trd2(LAS unsigned char* a0, LAS unsigned char* a1) {
  const v2u lo = trd(a0), hi = trd(a1);
  return v4u{lo.x, lo.y, hi.x, hi.y};
}
__device__ __forceinline__ f32x4 sel4(int sub, f32x4 a0, f32x4 a1, f32x4 a2, f32x4 a3) {
  const f32x4 lo = (sub & 1) ? a1 : a0, hi = (sub & 1) ? a3 : a2;
  return (sub & 2) ? hi : lo;
}
__device__ __forceinline__ v4u tag4(f32x4 a, unsigned tag) {
  v4u r;
#pragma unroll
  for (int k = 0; k < 4; ++k) r[k] = ((__float_as_uint(a[k]) + 0x80u) & 0xffffff00u) | ((tag >> (8 * k)) & 0xffu);
  return r;
}
__device__ __forceinline__ bool all_ok(unsigned acc) { return __builtin_amdgcn_ballot_w64((acc & 0xffu) != 0u) == 0ull; }
__device__ __forceinline__ bool all_zero(unsigned acc) { return __builtin_amdgcn_ballot_w64(acc != 0u) == 0ull; }
__device__ __forceinline__ void glds_s(const void* sbase, unsigned voff, unsigned ldst) {
  asm volatile(SAFE_NOP "s_mov_b32 m0, %2\n\ts_nop 0\n\tglobal_load_lds_dwordx4 %0, %1 nt" :: "v"(voff), "s"(sbase), "s"(ldst) : "memory");
}
__device__ __forceinline__ void glds_sd(const void* sbase, unsigned voff, unsigned ldst) {
  asm volatile("s_mov_b32 m0, %2\n\ts_nop 0\n\tglobal_load_lds_dwordx4 %0, %1" :: "v"(voff), "s"(sbase), "s"(ldst) : "memory");
}
__device__ __forceinline__ void glds_v(const void* vaddr, unsigned ldst) {
  asm volatile(SAFE_NOP "s_mov_b32 m0, %1\n\ts_nop 0\n\tglobal_load_lds_dwordx4 %0, off" :: "v"(vaddr), "s"(ldst) : "memory");
}
__device__ __forceinline__ void wait_stage(LAS unsigned char* L, unsigned want) {
#pragma unroll 1
  for (unsigned spins = 0; spins < SPIN_CAP; ++spins) {
    const v4u f = *(volatile LAS v4u*)(L + L_FL);
    const unsigned m = min(min(f.x, f.y), min(f.z, f.w));
    if (__builtin_amdgcn_readfirstlane(m) >= want) break;
    __builtin_amdgcn_s_sleep(2);
  }
  MEMFENCE();
}
__device__ __forceinline__ void wait_all(LAS unsigned char* a, unsigned want) {
#pragma unroll 1
  for (unsigned spins = 0; spins < SPIN_CAP; ++spins) {
    const v4u f = *(volatile LAS v4u*)a;
    if (__builtin_amdgcn_readfirstlane(min(min(f.x, f.y), min(f.z, f.w))) >= want) break;
  }
  MEMFENCE();
}
__device__ __forceinline__ v4u ln_row(v4u v, float live) {
  f32x2 x[4];
#pragma unroll
  for (int i = 0; i < 4; ++i) x[i] = f32x2{bflo(v[i]), bfhi(v[i])};
  const float x0 = __builtin_bit_cast(float, __builtin_amdgcn_readfirstlane(__builtin_bit_cast(int, x[0][0])));
  const f32x2 d0 = x[0] - x0, d1 = x[1] - x0, d2 = x[2] - x0, d3 = x[3] - x0;
  const f32x2 t1 = (d0 + d1) + (d2 + d3), t2 = (d0 * d0 + d1 * d1) + (d2 * d2 + d3 * d3);
  const float s1 = wave_sum((t1[0] + t1[1]) * live), s2 = wave_sum((t2[0] + t2[1]) * live);
  const float md = s1 * (1.0f / D);
  const float var = fmaxf(fmaf(-md, md, s2 * (1.0f / D)), 0.f);
  const float mb = rbf(x0 + md);
  const float inv = __builtin_amdgcn_rcpf(rbf(__builtin_amdgcn_sqrtf(rbf(rbf(var) + 1e-5f))));
  v4u o;
#pragma unroll
  for (int i = 0; i < 4; ++i) { const unsigned d = pk(x[i][0] - mb, x[i][1] - mb); o[i] = pk(bflo(d) * inv, bfhi(d) * inv); }
  return o;
}
__device__ __forceinline__ float rows_reduce4(f32x4 a) {
  auto p = __builtin_amdgcn_permlane32_swap(__float_as_uint(a[0]), __float_as_uint(a[2]), false, false);
  auto q = __builtin_amdgcn_permlane32_swap(__float_as_uint(a[1]), __float_as_uint(a[3]), false, false);
  const float c = __uint_as_float(p[0]) + __uint_as_float(p[1]), d = __uint_as_float(q[0]) + __uint_as_float(q[1]);
  auto r = __builtin_amdgcn_permlane16_swap(__float_as_uint(c), __float_as_uint(d), false, false);
  return __uint_as_float(r[0]) + __uint_as_float(r[1]);
}
__device__ __forceinline__ unsigned or4(v4u v) { return (v.x | v.y) | (v.z | v.w); }
__device__ __forceinline__ f32x4 asf(v4u v) { return __builtin_bit_cast(f32x4, v); }
__device__ __forceinline__ unsigned tag1(float a, unsigned tb) { return ((__float_as_uint(a) + 0x80u) & 0xffffff00u) | tb; }
__device__ __forceinline__ float reduce_inbox(__amdgpu_buffer_rsrc_t rs, const int (&off)[4], v4u tg, v4u tgl, unsigned& cnt) {
  float r;
#pragma unroll 1
  for (cnt = 1;; ++cnt) {
    MEMFENCE();
    v4u v[4];
#pragma unroll
    for (int i = 0; i < 4; ++i) v[i] = __builtin_amdgcn_raw_buffer_load_b128(rs, off[i], 0, SC1);
    v[0] ^= tg; v[1] ^= tg; v[2] ^= tg; v[3] ^= tgl;
    r = rows_reduce4((asf(v[0]) + asf(v[1])) + (asf(v[2]) + asf(v[3])));
    r += dpp<ROR4>(r); r += dpp<ROR8>(r);
    PIN(r);
    if (__builtin_expect(all_ok(or4(v[0]) | or4(v[1]) | or4(v[2]) | or4(v[3])), 1)) break;
    MEMFENCE();
    if (cnt > SPIN_CAP) break;
  }
  return r;
}

__device__ __forceinline__ int inbox(int x, int b, int t) { return ((x & 3) * 8 + b * 2 + (t >> 1)) * 4 + (x >> 2) * 2 + (t & 1); }
#define LT_VO ((lch ^ (((lrow >> 1) & 1) << 1)) << 4)
#define NOW() ((unsigned)__builtin_amdgcn_s_memrealtime() & 0x3fffffffu)
#define WAIT_UNTIL(dt) do { const unsigned tg_ = t0 + (dt) - 1u; while (((tg_ - NOW()) & 0x3fffffffu) < 2000u) {} MEMFENCE(); } while (0)

template <int W> __device__ __forceinline__ void stream_kv(const char* src, const char* vsrc, unsigned dst, unsigned (&vo)[4], unsigned ve, unsigned flag_addr,
                                                     bool rep, LAS unsigned char* L, __amdgpu_buffer_rsrc_t rs, int toff, unsigned thr) {
#pragma unroll 1
  for (int it = 1; it < 8; ++it) {
    if (it == 4 && rep) {
      wait_all(L + L_FL + 32, 1);
      const unsigned now4 = NOW() * 4u, from = (int)(now4 - thr) > 0 ? now4 : thr;
      const unsigned t = (now4 >> 2) + ((from - now4 + 15u * PACE) >> 2);
      __builtin_amdgcn_raw_buffer_store_b64(v2u{t, *(LAS unsigned*)(L + L_FL + 48)}, rs, toff, 0, SC1);
    }
    if (it == 4) { src = vsrc; vo[0] = ve; vo[1] = ve ^ 32u; vo[2] = ve; vo[3] = ve ^ 32u; }
#pragma unroll
    for (int i = 0; i < 4; ++i) {
      if (PACE) { while ((int)(NOW() * 4u - thr) < 0) {} thr += PACE; }
      asm volatile("s_waitcnt vmcnt(%0)" :: "n"(W - 1) : "memory");
      glds_s(src, vo[i], dst);
      src += 32768; dst += 4096;
      if (i == W - 5) asm volatile("ds_write_b32 %0, %1" :: "v"(flag_addr), "v"(it - 1) : "memory");
    }
  }
}

__device__ __forceinline__ unsigned time_base(LAS unsigned char* L, __amdgpu_buffer_rsrc_t rs, int lane, unsigned tag) {
  v2u tz = *(volatile LAS v2u*)(L + L_EP + (lane & 7) * 16);
  if (__builtin_expect(!all_zero(tz.y ^ tag), 0)) {
#pragma unroll 1
    for (unsigned spins = 0; spins < SPIN_CAP; ++spins) {
      MEMFENCE();
      tz = __builtin_amdgcn_raw_buffer_load_b64(rs, WS_T0 + (lane & 7) * 128, 0, SC1);
      PIN(tz);
      if (all_zero(tz.y ^ tag)) break;
      __builtin_amdgcn_s_sleep(2);
    }
  }
  const unsigned r0 = __builtin_amdgcn_readfirstlane(tz.x);
  float e = (float)((int)((tz.x - r0) << 2) >> 2);
  e = fmaxf(e, dpp<ROR1>(e)); e = fmaxf(e, dpp<ROR2>(e)); e = fmaxf(e, dpp<ROR4>(e));
  return r0 + (unsigned)__builtin_amdgcn_readfirstlane((int)e);
}
__device__ __forceinline__ unsigned time_load(const void* a) {
  u64 t0, t1; unsigned v;
  asm volatile("s_memtime %0\n\ts_waitcnt lgkmcnt(0)\n\ts_load_dword %2, %3, 0x0\n\ts_waitcnt lgkmcnt(0)\n\ts_memtime %1\n\ts_waitcnt lgkmcnt(0)"
               : "=&s"(t0), "=&s"(t1), "=&s"(v) : "s"(a) : "memory");
  return (unsigned)t1 - (unsigned)t0;
}
__global__ void __launch_bounds__(512, 2) fused_decode_block(Params) {
  __shared__ __attribute__((aligned(16))) unsigned char smem[L_END];
  LAS unsigned char* L = (LAS unsigned char*)smem;
  const unsigned lbase = (unsigned)(size_t)L;
  const int c = blockIdx.x, tid = threadIdx.x;
  const u64 kp = (u64)__builtin_amdgcn_kernarg_segment_ptr();
  u64 a0, a1, a2, a3, a4, a5, a6, a7, tclk;
  asm volatile(
      "s_load_dwordx16 s[4:19], %[kp], 0x0\n\t"
      "s_getpc_b64 s[20:21]\n\t"
      "s_mov_b32 m0, %[scr]\n\t"
      "s_and_b32 s22, %[c], -8\n\t"
      "v_lshrrev_b32 v1, 6, %[tid]\n\t"
      "v_add_u32 v1, s22, v1\n\t"
      "v_min_u32 v1, %[cb], v1\n\t"
      "v_lshlrev_b32 v1, 7, v1\n\t"
      "global_load_lds_dword v1, s[20:21]\n\t"
      "s_lshr_b32 s22, %[c], 3\n\t"
      "s_mul_i32 s22, s22, 14\n\t"
      "s_and_b32 s23, %[c], 1\n\t"
      "s_lshl_b32 s23, s23, 12\n\t"
      "s_and_b32 s26, %[c], 6\n\t"
      "s_lshl_b32 s26, s26, 9\n\t"
      "s_add_u32 s23, s23, s26\n\t"
      "v_lshrrev_b32 v2, 5, %[tid]\n\t"
      "v_min_u32 v2, 13, v2\n\t"
      "v_add_u32 v2, s22, v2\n\t"
      "v_lshl_add_u32 v2, v2, 13, s23\n\t"
      "v_and_b32 v1, 31, %[tid]\n\t"
      "v_lshl_add_u32 v2, v1, 5, v2\n\t"
      "s_waitcnt lgkmcnt(0)\n\t"
      "global_load_lds_dword v2, s[4:5]"
      : "=&{s[4:5]}"(a0), "=&{s[6:7]}"(a1), "=&{s[8:9]}"(a2), "=&{s[10:11]}"(a3), "=&{s[12:13]}"(a4), "=&{s[14:15]}"(a5), "=&{s[16:17]}"(a6),
        "=&{s[18:19]}"(a7), "=&{s[24:25]}"(tclk)
      : [kp] "s"(kp), [c] "s"(c), [tid] "v"(tid), [scr] "i"(L_EP + 1024), [cb] "i"(CODE_BYTES / 128)
      : "memory", "scc", "s20", "s21", "s22", "s23", "s26", "v1", "v2");
  SB();
  __builtin_assume(c >= 0 && c < 256);
  const unsigned tstart = (unsigned)tclk;
  const int lane = tid & 63, w = __builtin_amdgcn_readfirstlane(tid >> 6);
  const int kvh = ((c & 1) << 2) | ((c >> 1) & 3), j = c >> 3, b = j >> 3, s = j & 7;
  const int n = lane & 15, g = lane >> 4, vq = n >> 2, khalf = lane >> 5;
  const int lrow = lane >> 3, lch = lane & 7;
  STAMP_DECL;
  Params p;
  p.wq = (const u16*)a0; p.x = (const u16*)a1; p.ws = (unsigned char*)((a2 + WS_ALIGN) & ~(u64)WS_ALIGN);
  p.kc = (const u16*)a3; p.vc = (const u16*)a4; p.wg = (const u16*)a5; p.wu = (const u16*)a6; p.wo = (const u16*)a7;
#define LATE_ARG(off) ({ u64 v_; asm volatile("s_load_dwordx2 %0, %1, " #off "\n\ts_waitcnt lgkmcnt(0)" : "=s"(v_) : "s"(kp) : "memory"); v_; })
  const unsigned kvoff = ((unsigned)(b * 8 + kvh) * SK + (SK - WIN)) * 512u + (unsigned)(s + 8 * w) * 1024u;
  if (__builtin_expect(w >= 4, 1)) {
    __builtin_amdgcn_s_setprio(3);
    if (lane < 56) {
      asm volatile("s_mov_b32 m0, %2\n\ts_nop 0\n\tglobal_load_lds_dwordx4 %0, %1" :: "v"((w - 4) * 896 + lane * 16), "s"(p.x), "s"(lbase + L_XN + (w - 4) * XN_STR) : "memory");
      asm volatile("s_mov_b32 m0, %2\n\ts_nop 0\n\tglobal_load_lds_dword %0, %1 sc1" :: "v"(WS_EPOCH), "s"(p.ws), "s"(lbase + L_EP + (w - 4) * 256) : "memory");
    }
    if (Q_DELAY) __builtin_amdgcn_s_sleep(Q_DELAY);
    const unsigned qo = (unsigned)(b * 112 + 8 * (w - 4) + lrow) * 8192u + (kvh * 8 + s) * 128 + ((lch ^ (((lrow >> 1) & 3) << 1)) << 4);
#pragma unroll
    for (int i = 0; i < 4; ++i) {
      const int ii = i < 3 || w < 6 ? i : 2;
      glds_sd((const char*)p.wq + ii * 262144, qo, lbase + L_WQ + (w - 4) * 1024 + ii * 4096);
    }
  }
  if (TLB_WARM && w == 3) {
    asm volatile("s_mov_b32 m0, %5\n\ts_nop 0\n\tglobal_load_lds_dword %0, %2\n\tglobal_load_lds_dword %1, %3\n\tglobal_load_lds_dword %1, %4"
                 :: "v"(kvoff), "v"((lane & 1) * 1605628u), "s"(p.vc), "s"(p.wg), "s"(p.wu), "s"(lbase + L_EP + 1024) : "memory");
  }
  if (c == DET_WG && w == 0)
    asm volatile("global_store_dwordx4 %0, %1, %2 sc1\n\ts_nop 1" :: "v"(WS_PRB + (lane >> 5) * 8192 + (lane & 31) * 16), "v"(v4u{0u, 0u, 0u, 0u}), "s"(p.ws) : "memory");
  unsigned vo[4];
#pragma unroll
  for (int i = 0; i < 4; ++i) { vo[i] = (lane ^ (4 * i + w)) << 4; }
  *(LAS unsigned*)(L + L_FL + w * 4) = 0u;
  *(LAS unsigned*)(L + L_FL + 32 + w * 4) = 0u;
  WG_BARRIER();
  unsigned thr = 0;
  if (w < 4) {
    const unsigned tb0 = NOW();
#pragma unroll
    for (int i = 0; i < 4; ++i) glds_s((const char*)p.kc + kvoff + i * 32768, vo[i], lbase + L_K + w * 1024 + i * 4096);
    thr = (tb0 + PACE0) * 4u;
    VM_WAIT(4);
    const unsigned t1 = (NOW() - PACE1) * 4u;
    if ((int)(t1 - thr) > 0) thr = t1;
  }
  SB();
  STAMP(19);
  const __amdgpu_buffer_rsrc_t rs = __builtin_amdgcn_make_buffer_rsrc((void*)p.ws, 0, WS_END, 0x00020000);
  const bool ffn = j < 8 && kvh < 7;
  const int rsl = j & 7, cg = (j >> 3) - 1;
  const int nline = cg == 0 ? 3 : 2, line0 = cg == 0 ? 0 : 2 * cg + 1;
  const int f = kvh * 8 + j;

  const int vsw = (vq << 1) | (g & 1);
  unsigned v0 = lbase + L_V + (8 * g + vq) * 512 + (((2 * w) ^ vsw) << 5) + (n & 3) * 8;
  unsigned v1 = v0 ^ 32u;
  unsigned pa = lbase + L_P + (vq & 1) * P_STR + g * 16;
  int gcoff = WS_GC + (w * 64 + j) * 256 + lane * 4;
  int off0 = WS_GC + (rsl * 64 + 8 * w) * 256 + lane * 16;
  int so0 = WS_GS + (kvh * 32 + 8 * w + g) * 16 + (vq & 1) * 8;
  unsigned oa = lbase + L_OB + w * OB_STR + ((vq & 1) * 32 + (vq >> 1) * 16 + 4 * (n & 3) + g) * 2;
  unsigned wo0 = (kvh * 512 + rsl * 32 + lrow) * 896 + (line0 + w - 4) * 128 + LT_VO, wo1 = (wo0 ^ 64) + 8 * 896;
  PIN(wo0); PIN(wo1);
#define WO_ISSUE() do { _Pragma("unroll") for (int i_ = 0; i_ < 8; ++i_) \
    glds_s((const char*)p.wo + ((i_ & 2) * 8 + (i_ >> 2) * 256) * 896, (i_ & 1) ? wo1 : wo0, lbase + L_K + (w - 3) * 16384 + i_ * 1024); } while (0)
  const char* pb = ((lch & 4) ? (const char*)p.wu : (const char*)p.wg) + (size_t)(8 * (cg == 0 ? 40 + j : j < 8 ? 2 * j : 2 * j - 16) + lrow) * (FF * 2) +
                   kvh * 512 + (lch & 3) * 128;
  PIN(pb);
#define PF_ISSUE() do { \
    asm volatile("s_mov_b32 m0, %1\n\ts_nop 0\n\tglobal_load_lds_dword %0, off" :: "v"(pb), "s"(lbase + L_EP + 1024) : "memory"); \
    if (cg != 0) asm volatile("s_mov_b32 m0, %1\n\ts_nop 0\n\tglobal_load_lds_dword %0, off" :: "v"(pb + 8 * FF * 2), "s"(lbase + L_EP + 1024) : "memory"); } while (0)
  PIN(v0); PIN(v1); PIN(pa); PIN(gcoff); PIN(off0); PIN(so0); PIN(oa);

  if (__builtin_expect(w < 4, 1)) {
    const char* src = (const char*)p.kc + kvoff + 4 * 32768;
    const char* vsrc = (const char*)p.vc + kvoff;
    const unsigned ve = (lane ^ ((((w & 1) << 2) | (khalf << 1)) << 1)) << 4;
    const unsigned flag_addr = lbase + L_FL + w * 4;
    stream_kv<LWIN>(src, vsrc, lbase + L_K + w * 1024 + 4 * 4096, vo, ve, flag_addr, j == 0 && w == 0, L, rs, lane == 0 ? WS_T0 + kvh * 128 : WS_END, thr);
    asm volatile("ds_write_b32 %0, %1" :: "v"(flag_addr), "v"(7) : "memory");
    STAMP(3);
  } else {
    const int qt = (w + 1) & 3;
    unsigned tag, tq;
    {
      unsigned xa = lbase + L_XN + (w - 4) * XN_STR + (lane < 56 ? lane : 55) * 16;
      float live = lane < 56 ? 1.f : 0.f;
      const int row = 4 * g + vq, sw = (row >> 1) & 3;
      unsigned wa = lbase + L_WQ + row * 128 + (((w - 4) ^ sw) << 5) + (n & 3) * 8;
      unsigned xb = lbase + L_XN + vq * XN_STR + (b * 112 + 4 * g) * 2;
      int off = WS_GQ + (((kvh * 4 + g) * 4 + b) * 512 + s * 64 + (w - 4) * 16 + n) * 8;
      PIN(xa); PIN(live); PIN(wa); PIN(xb); PIN(off);
      VM_WAIT(4);
      const v4u xr = *(LAS v4u*)(size_t)xa;
      tag = __builtin_amdgcn_readfirstlane(*(LAS unsigned*)(L + L_EP + (w - 4) * 256)) + 0x01010101u;
      *(LAS unsigned*)(L + L_FL + 48) = tag;
      *(LAS v4u*)(size_t)xa = ln_row(xr, live);
      VM_WAIT(0);
      *(volatile LAS unsigned*)(L + L_FL + 16 + w * 4) = 1u;
      STAMP(1);
      wait_all(L + L_FL + 32, 1);
      v2u wf[7], xf[7];
#pragma unroll
      for (int ks = 0; ks < 7; ++ks) { wf[ks] = trd((LAS unsigned char*)(size_t)(wa + ks * 2048)); xf[ks] = *(LAS v2u*)(size_t)(xb + ks * 32); }
      SB();
      f32x4 a = {0.f, 0.f, 0.f, 0.f};
#pragma unroll
      for (int ks = 0; ks < 7; ++ks) a = mfma16(xf[ks], wf[ks], a);
      __builtin_amdgcn_raw_buffer_store_b64(v2u{__float_as_uint(a[0]), tag}, rs, off, 0, SC1);
      tq = NOW();
      if (TLB_WARM && w == 5) {
        const u64 wdp = LATE_ARG(0x40);
        asm volatile("s_mov_b32 m0, %4\n\ts_nop 0\n\tglobal_load_lds_dword %0, %2\n\tglobal_load_lds_dword %1, %3"
                     :: "v"(min(lane, 2) * 1835004u), "v"((lane & 1) * 1605628u), "s"(p.wo), "s"(wdp), "s"(lbase + L_EP + 1024) : "memory");
      }
      STAMP(2);
    }
    v4u k0[8], k1[8];
#define K_FRAGS() do { wait_stage(L, 1 + qt); LAS unsigned char* kr_ = L + L_K + (32 * qt + 2 * n) * 512; \
      _Pragma("unroll") for (int t = 0; t < 8; ++t) { const int o_ = ((4 * t + g) ^ n) << 4; k0[t] = *(LAS v4u*)(kr_ + o_); k1[t] = *(LAS v4u*)(kr_ + 512 + o_); } \
      _Pragma("unroll") for (int t = 0; t < 8; ++t) { PIN(k0[t]); PIN(k1[t]); } } while (0)
    if (c == DET_WG && qt >= 2) {
      unsigned lo = 0xffffu, hi = 0xffffu;
#pragma unroll 1
      for (int k = 2 * qt - 4; k < 2 * qt - 2; ++k) { lo = min(lo, time_load(p.ws + WS_PRB + k * 128)); hi = min(hi, time_load(p.ws + WS_PRB + 8192 + k * 128)); }
      if (qt == 2) *(volatile LAS unsigned*)(L + L_FL + 52) = lo | (hi << 16);
      else {
        unsigned o = 0u;
#pragma unroll 1
        for (unsigned spins = 0; spins < SPIN_CAP && o == 0u; ++spins) o = __builtin_amdgcn_readfirstlane(*(volatile LAS unsigned*)(L + L_FL + 52));
        const unsigned xr0 = (__builtin_amdgcn_s_getreg(20 | (3 << 11)) & 3) | (min(lo, o & 0xffffu) < min(hi, o >> 16) ? 0u : 4u);
        if (lane == 0) __builtin_amdgcn_raw_buffer_store_b32((tag & ~7u) | ((xr0 - DET_WG) & 7u), rs, WS_ROT, 0, SC1);
      }
    }
    if (qt >= 2) K_FRAGS();
    else {
      int gqoff = WS_GQ + ((kvh * 4 + b) * 4) * 4096 + qt * 2048 + lane * 16;
      PIN(gqoff);
      while (((NOW() - tq) & 0x3fffffffu) < D_QG) __builtin_amdgcn_s_sleep(1);
      STAMP(16);
      unsigned qp[2];
#pragma unroll 1
      for (unsigned spins = 0;; ) {
        MEMFENCE();
        v4u v[8];
#pragma unroll
        for (int i = 0; i < 8; ++i) v[i] = __builtin_amdgcn_raw_buffer_load_b128(rs, gqoff + (i & 1) * 1024, (i >> 1) * 4096, SC1);
        SB();
        K_FRAGS();
        SB();
        unsigned bad = 0u;
#pragma unroll
        for (int i = 0; i < 8; ++i) bad |= (v[i].y ^ tag) | (v[i].w ^ tag);
#pragma unroll
        for (int m = 0; m < 2; ++m)
          qp[m] = pk((__uint_as_float(v[m].x) + __uint_as_float(v[2 + m].x)) + (__uint_as_float(v[4 + m].x) + __uint_as_float(v[6 + m].x)),
                     (__uint_as_float(v[m].z) + __uint_as_float(v[2 + m].z)) + (__uint_as_float(v[4 + m].z) + __uint_as_float(v[6 + m].z)));
        COUNT(22, spins + 1);
        if (all_zero(bad) || ++spins > SPIN_CAP) break;
      }
#pragma unroll
      for (int m = 0; m < 2; ++m) *(LAS unsigned*)(L + L_Q + qt * Q_STR + m * 256 + lane * 4) = qp[m];
      MEMFENCE();
      *(volatile LAS unsigned*)(L + L_FL + w * 4) = 1u;
      STAMP(4);
    }
#pragma unroll 1
    for (unsigned spins = 0; spins < 4 * SPIN_CAP; ++spins) {
      const v4u fl = *(volatile LAS v4u*)(L + L_FL + 16);
      if (__builtin_amdgcn_readfirstlane(min(fl.x, fl.w)) != 0u) break;
    }
    MEMFENCE();
    {
      v4u qf[8];
#pragma unroll
      for (int t = 0; t < 8; ++t) qf[t] = *(LAS v4u*)(L + L_Q + (n & 1) * Q_STR + (4 * t + g) * 16);
      SB();
      f32x4 a0 = {0.f, 0.f, 0.f, 0.f}, a1 = a0;
#pragma unroll
      for (int t = 0; t < 8; ++t) { a0 = mfma32(qf[t], k0[t], a0); a1 = mfma32(qf[t], k1[t], a1); }
      *(LAS v2u*)(L + L_SC + (16 * qt + n) * 8) = v2u{pk(a0[0], a1[0]), pk(a0[1], a1[1])};
      MEMFENCE();
    }
    *(volatile LAS unsigned*)(L + L_FL + w * 4) = 2u;
    if (w >= 6) {
      wait_all(L + L_FL + 16, 2);
      const v2u sc = *(LAS v2u*)(L + L_SC + lane * 8);
      const float s00 = bflo(sc.x) * 0.0625f, s01 = bfhi(sc.x) * 0.0625f, s10 = bflo(sc.y) * 0.0625f, s11 = bfhi(sc.y) * 0.0625f;
      const float m0 = wave_max(fmaxf(s00, s01)), m1 = wave_max(fmaxf(s10, s11));
      const float e00 = __builtin_amdgcn_exp2f((s00 - m0) * LOG2E), e01 = __builtin_amdgcn_exp2f((s01 - m0) * LOG2E);
      const float e10 = __builtin_amdgcn_exp2f((s10 - m1) * LOG2E), e11 = __builtin_amdgcn_exp2f((s11 - m1) * LOG2E);
      if (w == 7) {
        *(LAS unsigned*)(L + L_P + lane * 4) = pk(e00, e01);
        *(LAS unsigned*)(L + L_P + P_STR + lane * 4) = pk(e10, e11);
        STAMP(5);
      } else {
        const float l0 = wave_sum(e00 + e01), l1 = wave_sum(e10 + e11);
        if (lane == 0) __builtin_amdgcn_raw_buffer_store_b128(tag4(f32x4{m0, l0, m1, l1}, tag), rs, WS_GS + (kvh * 32 + j) * 16, 0, SC1);
      }
    }
    if (w == 4) {
      if (lane < 9) asm volatile("s_mov_b32 m0, %2\n\ts_nop 0\n\tglobal_load_lds_dwordx4 %0, %1 sc1" :: "v"(WS_T0 + lane * 128), "s"(p.ws), "s"(lbase + L_EP) : "memory");
    }
    if (WO_POS == 0 && j >= 8 && w - 4 < nline) { wait_stage(L, 7); WO_ISSUE(); }
  }

#define VFRAG(base, t) trd2((LAS unsigned char*)(size_t)((base) + (t) * 16384), (LAS unsigned char*)(size_t)((base) + (t) * 16384 + 2048))
  wait_stage(L, 6);
  v4u a00 = VFRAG(v0, 0), a10 = VFRAG(v1, 0), a01 = VFRAG(v0, 1), a11 = VFRAG(v1, 1);
  SB();
  if (w < 4) VM_WAIT(4);
  STAMP(18);
  WG_BARRIER();
  v4u pf[4];
#pragma unroll
  for (int t = 0; t < 4; ++t) pf[t] = *(LAS v4u*)(size_t)(pa + t * 64);
  const v4u a02 = VFRAG(v0, 2), a12 = VFRAG(v1, 2);
  const unsigned tt = *(LAS unsigned*)(L + L_FL + 48);
  SB();
  f32x4 o0 = {0.f, 0.f, 0.f, 0.f}, o1 = o0;
  o0 = mfma32(pf[0], a00, o0); o1 = mfma32(pf[0], a10, o1);
  o0 = mfma32(pf[1], a01, o0); o1 = mfma32(pf[1], a11, o1);
  o0 = mfma32(pf[2], a02, o0); o1 = mfma32(pf[2], a12, o1);
  unsigned tb = (tt >> (8 * (n & 3))) & 0xffu;
  PIN(tb);
  const unsigned tag = __builtin_amdgcn_readfirstlane(tt);
  SB();
  if (PF_POS == 0 && w == 7 && kvh < 7) { __builtin_amdgcn_s_sleep(PF_SLEEP); PF_ISSUE(); }
  if (w < 4) VM_WAIT(0);
  STAMP(6);
  WG_BARRIER();
  const unsigned rotv = *(volatile LAS unsigned*)(L + L_EP + 128);
  const v4u a03 = VFRAG(v0, 3), a13 = VFRAG(v1, 3);
  SB();
  unsigned rot = __builtin_amdgcn_readfirstlane(rotv);
  if (__builtin_expect((rot ^ tag) > 7u, 0)) {
#pragma unroll 1
    for (unsigned spins = 0; spins < SPIN_CAP; ++spins) {
      MEMFENCE();
      rot = __builtin_amdgcn_readfirstlane(__builtin_amdgcn_raw_buffer_load_b32(rs, WS_ROT, 0, SC1));
      if ((rot ^ tag) <= 7u) break;
      __builtin_amdgcn_s_sleep(2);
    }
  }
  const int xr = (c + rot) & 7, half = xr >> 2;
  const int gcs = ((xr & 3) * 16 + half) * 8192;
  o0 = mfma32(pf[3], a03, o0); o1 = mfma32(pf[3], a13, o1);
  __builtin_amdgcn_raw_buffer_store_b32(tag1(khalf ? o1[0] : o0[0], tb), rs, gcoff, gcs, SC1);
  STAMP(7);
  if (PF_POS == 1 && w == 7 && kvh < 7) { __builtin_amdgcn_s_sleep(2); PF_ISSUE(); }
#define PRE_WRITE(val, o) do { if (PREWRITE) __builtin_amdgcn_raw_buffer_store_b32((val) ^ 0x80u, rs, (o), 0, SC1); } while (0)

  const int tsw = ((vq >> 1) & 1) | ((g & 1) << 1);
#define LT_RADDR(base, ks, t, h) ((base) + (32 * (ks) + 8 * g + 4 * (h) + vq) * 128 + (((t) ^ tsw) << 5) + (n & 3) * 8)

  if (j >= 8) {
    if (w < 4) {
      v4u tg = {tag & 0xffu, (tag >> 8) & 0xffu, (tag >> 16) & 0xffu, tag >> 24};
      v2u ts2 = (vq & 1) ? v2u{tg.z, tg.w} : v2u{tg.x, tg.y};
      int off1 = off0 + 1024, so1 = so0 + 64;
      PIN(off1); PIN(so1); PIN(tg); PIN(ts2);
      const unsigned t0 = time_base(L, rs, lane, tag);
      STAMP(16);
      WAIT_UNTIL(T_HC - D_ST);
#pragma unroll 1
      for (unsigned spins = 1;; ++spins) {
        MEMFENCE();
        v2u s0 = __builtin_amdgcn_raw_buffer_load_b64(rs, so0, 0, SC1);
        v2u s1 = __builtin_amdgcn_raw_buffer_load_b64(rs, so1, 0, SC1);
        WAIT_UNTIL(T_HC);
        v4u u0 = __builtin_amdgcn_raw_buffer_load_b128(rs, off0, gcs, SC1);
        v4u u1 = __builtin_amdgcn_raw_buffer_load_b128(rs, off1, gcs, SC1);
        SB();

        s0 ^= ts2; s1 ^= ts2;
        const float m0 = __uint_as_float(s0.x), m1 = __uint_as_float(s1.x);
        const float M = x32_max(x16_max(fmaxf(m0, m1)));
        float e0 = __builtin_amdgcn_exp2f((m0 - M) * LOG2E), e1 = __builtin_amdgcn_exp2f((m1 - M) * LOG2E);
        const float inv = __builtin_amdgcn_rcpf(x32_sum(x16_sum(__uint_as_float(s0.y) * e0 + __uint_as_float(s1.y) * e1)));
        e0 *= inv; e1 *= inv;
        unsigned bad = (s0.x | s0.y) | (s1.x | s1.y);
        PIN(e0); PIN(e1); PIN(bad);
        SB();
        u0 ^= tg; u1 ^= tg;
        const float o = rows_reduce4(asf(u0) * e0 + asf(u1) * e1);
        *(LAS u16*)(size_t)oa = (u16)pk(o, o);
        COUNT(21, spins);
        if (__builtin_expect(all_ok(bad | or4(u0) | or4(u1)), 1)) break;
        MEMFENCE();
        if (spins > SPIN_CAP) break;
      }
      STAMP(8);
      WG_BARRIER();
    } else if (w - 4 < nline) {
      int off[4];
#pragma unroll
      for (int t4 = 0; t4 < 4; ++t4) { off[t4] = WS_G1 + (inbox(line0 + w - 4, g, t4) * 64 + kvh * 8 + rsl) * 64 + n * 4; PIN(off[t4]); }
      VM_WAIT(1);
      STAMP(17);
#pragma unroll
      for (int t4 = 0; t4 < 4; ++t4) PRE_WRITE(tb, off[t4]);
      LAS unsigned char* kw = L + L_K + (w - 3) * 16384;
      v4u wf[2][4];
#pragma unroll
      for (int ks = 0; ks < 2; ++ks)
#pragma unroll
        for (int t4 = 0; t4 < 4; ++t4) wf[ks][t4] = trd2(LT_RADDR(kw, ks, t4, 0), LT_RADDR(kw, ks, t4, 1));
      unsigned ba = lbase + L_OB + vq * OB_STR + g * 16;
      PIN(ba);
      SB();
      WG_BARRIER();
      const v4u b0 = *(LAS v4u*)(size_t)ba, b1 = *(LAS v4u*)(size_t)(ba + 64);
      SB();
      f32x4 acc[4];
#pragma unroll
      for (int t4 = 0; t4 < 4; ++t4) acc[t4] = mfma32(b0, wf[0][t4], f32x4{0.f, 0.f, 0.f, 0.f});
#pragma unroll
      for (int t4 = 0; t4 < 4; ++t4) acc[t4] = mfma32(b1, wf[1][t4], acc[t4]);
      SB();
#pragma unroll
      for (int t4 = 0; t4 < 4; ++t4) __builtin_amdgcn_raw_buffer_store_b32(tag1(acc[t4][0], tb), rs, off[t4], 0, SC1);
      STAMP(9);
    } else if (w == 7 && j >= 16 && xr < 7) {
      const int r = ((j - 16) >> 2) * 28 + xr * 4 + (j & 3);
      const int ib = inbox(xr, (j - 16) >> 2, j & 3);
      const int col = r * 16 + 4 * (lane & 3) + g;
      float xin = bflo(*(const GAS u16*)(p.x + col));
      int o1[4], o3[4];
#pragma unroll
      for (int i = 0; i < 4; ++i) {
        o1[i] = WS_G1 + ib * 4096 + i * 1024 + lane * 16;
        o3[i] = (i == 3 && lane >= 32) ? WS_END : WS_G3 + ib * 4096 + i * 1024 + lane * 16;
        PIN(o1[i]); PIN(o3[i]);
      }
      int o2 = n < 8 ? WS_G2 + vq * 8192 + r * 64 + (g * 4 + (n & 3)) * 4 : WS_END;
      v4u tg = {tag & 0xffu, (tag >> 8) & 0xffu, (tag >> 16) & 0xffu, tag >> 24};
      v4u tgl = lane >= 32 ? v4u{0u, 0u, 0u, 0u} : tg;
      unsigned t16 = (tag >> ((n & 1) * 16)) & 0xffffu;
      GAS u16* op = (GAS u16*)LATE_ARG(0x48) + col;
      if (OUT_TOUCH == 1) asm volatile("s_mov_b32 m0, %1\n\ts_nop 0\n\tglobal_load_lds_ushort %0, off" :: "v"(op), "s"(lbase + L_EP + 1024) : "memory");
      if (OUT_TOUCH == 2 && n < 4) __hip_atomic_store(op, (u16)0, __ATOMIC_RELAXED, __HIP_MEMORY_SCOPE_AGENT);
      PIN(o2); PIN(xin); PIN(tg); PIN(tgl); PIN(t16); PIN(op);
      unsigned t0 = time_base(L, rs, lane, tag);
      PRE_WRITE(t16, o2);
      WG_BARRIER();
      unsigned cnt;
      WAIT_UNTIL(T_H1);
      const float at = reduce_inbox(rs, o1, tg, tg, cnt);
      const unsigned hb = pk(0.f, xin + rbf(at));
      __builtin_amdgcn_raw_buffer_store_b32(hb | t16, rs, o2, 0, SC1);
      COUNT(20, cnt);
      if (r == 0 && lane == 0) __builtin_amdgcn_raw_buffer_store_b32(tag, rs, WS_EPOCH, 0, SC1);
      STAMP(10);
      if (SHADOW) {
        WAIT_UNTIL(T_H2 + 30);
        unsigned tries = 0;
#pragma unroll 1
        for (;; ++tries) {
          MEMFENCE();
          const unsigned a0 = __builtin_amdgcn_raw_buffer_load_b32(rs, WS_G2 + half * 8192 + lane * 64, 0, SC1);
          const unsigned a1 = __builtin_amdgcn_raw_buffer_load_b32(rs, WS_G2 + half * 8192 + (lane < 48 ? lane + 64 : lane) * 64, 0, SC1);
          if (all_zero(((a0 ^ tag) | (a1 ^ tag)) & 0xffffu) || tries > SPIN_CAP) break;
        }
        if (tries) t0 = NOW() - (T_H2 + D_SH);
      } else if (cnt > 1) t0 = NOW() - (T_H1 + 50 - D_LATE);
      WAIT_UNTIL(T_H3);
      const float y = reduce_inbox(rs, o3, tg, tgl, cnt);
      const float res = __uint_as_float(hb) + rbf(y);
      if (OUT_SC1) { if (n < 4) __hip_atomic_store(op, (u16)pk(res, res), __ATOMIC_RELAXED, __HIP_MEMORY_SCOPE_AGENT); }
      else if (n < 4) *op = (u16)pk(res, res);
      COUNT(23, cnt);
      STAMP(11);
    }
  } else if (ffn) {
    WG_BARRIER();
    if (w == 7) { *(LAS v4u*)(L + L_WQ + lane * 16) = v4u{0x3f803f80u, 0x3f803f80u, 0x3f803f80u, 0x3f803f80u}; }
    const unsigned t0 = time_base(L, rs, lane, tag);
    p.wd = (const u16*)LATE_ARG(0x40);
    int off3[4];
    {
      const int pos = 8 * (w & 1) + lrow;
      const char* gub = ((lch & 4) ? (const char*)p.wu : (const char*)p.wg) + (size_t)(16 * (w >> 1) + 4 * (pos & 3) + (pos >> 2)) * (FF * 2) + f * 64 +
                        ((((lch & 3) ^ ((lrow >> 1) & 1)) ^ ((w & 1) << 1)) << 4);
      WAIT_UNTIL(T_HC - D_WD);
      if (w < 7) {
        const char* wdb = (const char*)p.wd + (size_t)(f * 32) * 896 + w * 128;
#pragma unroll
        for (int i = 0; i < 4; ++i) glds_s(wdb + i * 8 * 896, lrow * 896 + (LT_VO ^ ((i & 1) << 6)), lbase + L_WD + w * 4096 + i * 1024);
      }
#pragma unroll
      for (int t4 = 0; t4 < 4; ++t4) {
        off3[t4] = w < 7 ? WS_G3 + (inbox(w, g, t4) * 64 + f) * 64 + n * 4 : WS_END;
        PIN(off3[t4]);
        if (PW3 == 0) PRE_WRITE(tb, off3[t4]);
      }
      WAIT_UNTIL(T_GU);
#pragma unroll
      for (int i = 0; i < 7; ++i) glds_v(gub + (size_t)i * (64 * FF * 2), lbase + L_GU + (8 * i + w) * 1024);
    }
    if (PW3 == 1) {
#pragma unroll
      for (int t4 = 0; t4 < 4; ++t4) PRE_WRITE(tb, off3[t4]);
      VM_WAIT(4);
    } else
    VM_WAIT(0);
    WG_BARRIER();
    STAMP(12);
    if (PW3 == 2) {
#pragma unroll
      for (int t4 = 0; t4 < 4; ++t4) PRE_WRITE(tb, off3[t4]);
    }
    v4u df[4];
    {
      LAS unsigned char* dw = L + L_WD + (w < 7 ? w : 6) * 4096;
#pragma unroll
      for (int t4 = 0; t4 < 4; ++t4) {
        df[t4] = trd2(LT_RADDR(dw, 0, t4, 0), LT_RADDR(dw, 0, t4, 1));
      }
    }
    unsigned aa = lbase + L_ACT + vq * 64 + g * 16;
    PIN(aa);
    if (w < 4) {
      v4u gf[14];
      LAS unsigned char* gb = L + L_GU + (8 * g + vq) * 128 + ((n & 2) << 5) + ((w ^ tsw) << 4) + (n & 1) * 8;
#pragma unroll
      for (int ks = 0; ks < 14; ++ks) gf[ks] = trd2(gb + ks * 4096, gb + ks * 4096 + 512);
      unsigned hb0 = lbase + ((n & 3) == 1 ? L_WQ + (g ^ 2) * 16 : L_XN + vq * XN_STR + g * 16);
      unsigned wa = lbase + (n < 8 ? L_ACT + g * 64 + (8 * w + n) * 2 : L_Q + 16), sa = lbase + L_ST + g * 8;
      PIN(hb0); PIN(wa); PIN(sa);
      SB();
      WG_BARRIER();
      v4u hf[14];
#pragma unroll
      for (int ks = 0; ks < 14; ++ks) hf[ks] = *(LAS v4u*)(size_t)(hb0 + ks * 64);
      SB();
      f32x4 a = {0.f, 0.f, 0.f, 0.f};
#pragma unroll
      for (int ks = 0; ks < 10; ++ks) a = mfma32(hf[ks], gf[ks], a);
      SB();
      WG_BARRIER();
      const f32x2 st = *(LAS f32x2*)(size_t)sa;
      SB();
#pragma unroll
      for (int ks = 10; ks < 14; ++ks) a = mfma32(hf[ks], gf[ks], a);
      const float gt = rbf((a[0] - st[0] * a[1]) * st[1]);
      const float up = dpp<ROR8>(gt);
      const float av = rbf(gt * __builtin_amdgcn_rcpf(1.0f + __builtin_amdgcn_exp2f(-gt * LOG2E))) * up;
      *(LAS u16*)(size_t)wa = (u16)pk(av, av);
      STAMP(14);
    } else {
      int ho0 = lane < 56 ? WS_G2 + half * 8192 + (w - 4) * 1792 + lane * 16 : WS_END, ho1 = ho0 + 896;
      unsigned w0 = lbase + (lane < 56 ? L_XN + (w - 4) * XN_STR + lane * 8 : L_Q);
      unsigned sa = lbase + (lane == 32 ? L_ST + (w - 4) * 8 : L_Q);
      const unsigned tl = lane < 56 ? tag : 0u;
      v4u t2 = {tl & 0xffffu, tl >> 16, tl & 0xffffu, tl >> 16};
      PIN(ho0); PIN(ho1); PIN(w0); PIN(sa); PIN(t2);
      v4u hp;
      WAIT_UNTIL(T_H2);
#pragma unroll 1
      for (unsigned spins = 1;; ++spins) {
        MEMFENCE();
        const v4u h0 = __builtin_amdgcn_raw_buffer_load_b128(rs, ho0, 0, SC1);
        const v4u h1 = __builtin_amdgcn_raw_buffer_load_b128(rs, ho1, 0, SC1);
        hp = v4u{__builtin_amdgcn_perm(h0.y, h0.x, 0x07060302u), __builtin_amdgcn_perm(h0.w, h0.z, 0x07060302u),
                 __builtin_amdgcn_perm(h1.y, h1.x, 0x07060302u), __builtin_amdgcn_perm(h1.w, h1.z, 0x07060302u)};
        *(LAS v2u*)(size_t)w0 = v2u{hp.x, hp.y};
        *(LAS v2u*)(size_t)(w0 + 448) = v2u{hp.z, hp.w};
        SB();
        if (__builtin_expect(all_zero((or4(h0 ^ t2) | or4(h1 ^ t2)) & 0xffffu), 1)) break;
        MEMFENCE();
        if (spins > SPIN_CAP) break;
      }
      STAMP(13);
      WG_BARRIER();
      float p1 = 0.f, p2 = 0.f;
#pragma unroll
      for (int i = 0; i < 4; ++i) { p1 = dot2(hp[i], 0x3f803f80u, p1); p2 = dot2(hp[i], hp[i], p2); }
      auto sw = __builtin_amdgcn_permlane32_swap(__float_as_uint(p1), __float_as_uint(p2), false, false);
      float z = __uint_as_float(sw[0]) + __uint_as_float(sw[1]);
      z += dpp<ROR8>(z); z += dpp<ROR4>(z); z += dpp<ROR2>(z); z += dpp<ROR1>(z);
      z = x16_sum(z) * (1.0f / D);
      const float md = __builtin_bit_cast(float, __builtin_amdgcn_readlane(__builtin_bit_cast(int, z), 0));
      const float var = fmaxf(fmaf(-md, md, z), 0.f);
      const float inv = __builtin_amdgcn_rcpf(rbf(__builtin_amdgcn_sqrtf(rbf(rbf(var) + 1e-5f))));
      *(LAS f32x2*)(size_t)sa = f32x2{rbf(md), inv};
      WG_BARRIER();
    }
    WG_BARRIER();
    {
      const v4u af = *(LAS v4u*)(size_t)aa;
      f32x4 acc[4];
#pragma unroll
      for (int t4 = 0; t4 < 4; ++t4) acc[t4] = mfma32(af, df[t4], f32x4{0.f, 0.f, 0.f, 0.f});
      SB();
#pragma unroll
      for (int t4 = 0; t4 < 4; ++t4) __builtin_amdgcn_raw_buffer_store_b32(tag1(acc[t4][0], tb), rs, off3[t4], 0, SC1);
      STAMP(15);
    }
  }
  STAMP_FLUSH();
}

extern "C" void kernel_launch(void* const* d_in, const int* in_sizes, int n_in, void* d_out, int out_size, void* d_ws, size_t ws_size,
                              hipStream_t stream) {
  Params p;
  p.x = (const u16*)d_in[0]; p.kc = (const u16*)d_in[1]; p.vc = (const u16*)d_in[2];
  p.wq = (const u16*)d_in[3]; p.wo = (const u16*)d_in[4];
  p.wg = (const u16*)d_in[6]; p.wu = (const u16*)d_in[7]; p.wd = (const u16*)d_in[8];
  p.out = (u16*)d_out; p.ws = (unsigned char*)d_ws;
  fused_decode_block<<<256, 512, 0, stream>>>(p);
}
```
